```python
import torch

def _softmax(x, axis=-1):
    return torch.softmax(x, dim=axis)
def _gelu(x):
    return torch.nn.functional.gelu(x, approximate="tanh")
def _silu(x):
    return torch.nn.functional.silu(x)
def _sigmoid(x):
    return torch.sigmoid(x)
def _layernorm(x, axis=-1, eps=1e-5):
    m = x.mean(dim=axis, keepdim=True)
    v = x.var(dim=axis, keepdim=True, unbiased=False)
    return (x - m) / torch.sqrt(v + eps)
def _scatter_add(tgt, idx, src, axis=0):
    return tgt.clone().index_add_(axis, idx, src)
def _segment_sum(src, seg, n):
    out = torch.zeros((n,) + src.shape[1:], dtype=src.dtype)
    return out.index_add_(0, seg, src)
def _pad(x, pw):
    flat = tuple(v for pair in reversed(pw) for v in pair)
    return torch.nn.functional.pad(x, flat)

kernel_name = 'sequential_preLN__linear_attn_block__swiglu_ffn__prompt'

def setup_inputs():

    _w = (lambda a: (lambda t: t.to(torch.bfloat16) if t.is_floating_point() else t)(torch.as_tensor(a)))
    def _A_setup():
        B, S, D = 1, 2048, 512
        return {
            "x": _w((lambda *s: torch.randn(*s, dtype=torch.bfloat16) * 0.05)(B, S, D)),
            "wqkv": _w((lambda *s: torch.randn(*s, dtype=torch.bfloat16) * 0.05)(D, 3 * D)),
            "wo": _w((lambda *s: torch.randn(*s, dtype=torch.bfloat16) * 0.05)(D, D)),
        }
    _A_in = _A_setup()

    def _B_setup():
        B, S, D, F = 1, 2048, 512, 1792
        return {
            "x": _w((lambda *s: torch.randn(*s, dtype=torch.bfloat16) * 0.05)(B, S, D)),
            "w_gate": _w((lambda *s: torch.randn(*s, dtype=torch.bfloat16) * 0.05)(D, F)),
            "w_up": _w((lambda *s: torch.randn(*s, dtype=torch.bfloat16) * 0.05)(D, F)),
            "w_down": _w((lambda *s: torch.randn(*s, dtype=torch.bfloat16) * 0.05)(F, D)),
        }
    _B_in = _B_setup()

    return {
        "A_x": _A_in["x"],
        "A_wqkv": _A_in["wqkv"],
        "A_wo": _A_in["wo"],
        "B_x": _B_in["x"],
        "B_w_gate": _B_in["w_gate"],
        "B_w_up": _B_in["w_up"],
        "B_w_down": _B_in["w_down"],
    }

def reference(A_x, A_wqkv, A_wo, B_x, B_w_gate, B_w_up, B_w_down):
    def _A_ref(x, wqkv, wo):
        B, S, D = x.shape
        H = 32; Dh = D // H
        qkv = (lambda x, *d: x.permute(*d))((x @ wqkv).reshape(B, S, 3, H, Dh), 2, 0, 3, 1, 4)
        q, k, v = qkv[0], qkv[1], qkv[2]
        phi = lambda t: torch.where(t > 0, t + 1, torch.exp(t)) + 1e-6
        pq, pk = phi(q), phi(k)
        kv = torch.einsum("bhsd,bhse->bhde", pk, v)
        z = torch.einsum("bhsd->bhd", pk)
        num = torch.einsum("bhqd,bhde->bhqe", pq, kv)
        den = torch.einsum("bhqd,bhd->bhq", pq, z)[..., None] + 1e-6
        o = num / den
        return (lambda x, *d: x.permute(*d))(o, 0, 2, 1, 3).reshape(B, S, D) @ wo

    def _B_ref(x, w_gate, w_up, w_down):
        g = _silu(x @ w_gate)
        u = x @ w_up
        return (g * u) @ w_down

    x = A_x
    h = x + _A_ref(_layernorm(x, axis=-1), A_wqkv, A_wo)
    h2 = h + _B_ref(_layernorm(h, axis=-1), B_w_gate, B_w_up, B_w_down)
    return h2
```

```cpp
#include <hip/hip_runtime.h>
#include <hip/hip_bf16.h>
typedef unsigned short u16;
__device__ __forceinline__ float b2f(u16 v){ return __uint_as_float(((unsigned)v)<<16); }
__device__ __forceinline__ u16 f2b(float f){ unsigned u=__float_as_uint(f); u += 0x7fffu + ((u>>16)&1u); return (u16)(u>>16);}
constexpr int S=2048, D=512, F=1792, H=32, DH=16;
__global__ void k_ln(const u16* x, u16* y){
  int row = blockIdx.x*4 + (threadIdx.x>>6); int lane = threadIdx.x&63;
  float v[8]; float s=0;
  for(int j=0;j<8;++j){ v[j]=b2f(x[row*D+lane*8+j]); s+=v[j]; }
  for(int o=32;o>0;o>>=1) s+=__shfl_xor(s,o);
  float m=s/D; float q=0;
  for(int j=0;j<8;++j){ float d=v[j]-m; q+=d*d; }
  for(int o=32;o>0;o>>=1) q+=__shfl_xor(q,o);
  float r=rsqrtf(q/D+1e-5f);
  for(int j=0;j<8;++j) y[row*D+lane*8+j]=f2b((v[j]-m)*r);
}
__global__ void k_gemm(const u16* A, const u16* W, const u16* res, u16* C, int N, int K){
  __shared__ float As[16][65]; __shared__ float Ws[16][65];
  int bm=blockIdx.y*64, bn=blockIdx.x*64; int tx=threadIdx.x&15, ty=threadIdx.x>>4;
  float acc[4][4]={};
  for(int k0=0;k0<K;k0+=16){
    for(int i=threadIdx.x;i<64*16;i+=256){ int r=i>>4,c=i&15; As[c][r]=b2f(A[(bm+r)*K+k0+c]); }
    for(int i=threadIdx.x;i<64*16;i+=256){ int r=i>>6,c=i&63; Ws[r][c]=b2f(W[(k0+r)*N+bn+c]); }
    __syncthreads();
    for(int k=0;k<16;++k){ float a[4],b[4]; for(int i=0;i<4;++i){a[i]=As[k][ty*4+i]; b[i]=Ws[k][tx*4+i];}
      for(int i=0;i<4;++i)for(int j=0;j<4;++j) acc[i][j]+=a[i]*b[j]; }
    __syncthreads();
  }
  for(int i=0;i<4;++i)for(int j=0;j<4;++j){ int r=bm+ty*4+i,c=bn+tx*4+j; float v=acc[i][j]; if(res) v=b2f(f2b(v))+b2f(res[r*N+c]); C[r*N+c]=f2b(v);}
}
__device__ __forceinline__ float phi(float t){ return (t>0.f? t+1.f : __expf(t)) + 1e-6f; }
__global__ void k_kv(const u16* qkv, float* kv){
  int h=blockIdx.x; int t=threadIdx.x; if(t>=272) return; int d = t<256? t>>4 : t-256; int e=t&15;
  float s=0; for(int r=0;r<S;++r){ float pk=phi(b2f(qkv[r*3*D+D+h*DH+d])); float v = t<256? b2f(qkv[r*3*D+2*D+h*DH+e]) : 1.f; s+=pk*v; }
  kv[h*272+t]=s;
}
__global__ void k_o(const u16* qkv, const float* kv, u16* o){
  int i=blockIdx.x*256+threadIdx.x; int r=i/D, c=i%D, h=c/DH, e=c%DH;
  float num=0,den=0; for(int d=0;d<DH;++d){ float pq=phi(b2f(qkv[r*3*D+h*DH+d])); num+=pq*kv[h*272+d*16+e]; den+=pq*kv[h*272+256+d]; }
  o[i]=f2b(num/(den+1e-6f));
}
__global__ void k_act(const u16* g, const u16* u, u16* a){
  int i=blockIdx.x*256+threadIdx.x; float gv=b2f(g[i]); a[i]=f2b(gv/(1.f+__expf(-gv))*b2f(u[i]));
}
extern "C" void kernel_launch(void* const* d_in, const int* in_sizes, int n_in,
                              void* d_out, int out_size, void* d_ws, size_t ws_size,
                              hipStream_t stream) {
  const u16* x=(const u16*)d_in[0]; const u16* wqkv=(const u16*)d_in[1]; const u16* wo=(const u16*)d_in[2];
  const u16* wg=(const u16*)d_in[4]; const u16* wu=(const u16*)d_in[5]; const u16* wd=(const u16*)d_in[6];
  char* ws=(char*)d_ws; u16* ln=(u16*)ws; u16* qkv=(u16*)(ws+(4<<20)); float* kv=(float*)(ws+(12<<20)); u16* o=(u16*)(ws+(13<<20));
  u16* h=(u16*)(ws+(16<<20)); u16* g=(u16*)(ws+(20<<20)); u16* u=(u16*)(ws+(28<<20)); u16* a=(u16*)(ws+(36<<20));
  k_ln<<<S/4,256,0,stream>>>(x,ln);
  k_gemm<<<dim3(3*D/64,S/64),256,0,stream>>>(ln,wqkv,nullptr,qkv,3*D,D);
  k_kv<<<H,320,0,stream>>>(qkv,kv);
  k_o<<<S*D/256,256,0,stream>>>(qkv,kv,o);
  k_gemm<<<dim3(D/64,S/64),256,0,stream>>>(o,wo,x,h,D,D);
  k_ln<<<S/4,256,0,stream>>>(h,ln);
  k_gemm<<<dim3(F/64,S/64),256,0,stream>>>(ln,wg,nullptr,g,F,D);
  k_gemm<<<dim3(F/64,S/64),256,0,stream>>>(ln,wu,nullptr,u,F,D);
  k_act<<<S*F/256,256,0,stream>>>(g,u,a);
  k_gemm<<<dim3(D/64,S/64),256,0,stream>>>(a,wd,h,(u16*)d_out,D,F);
}
```

```cpp
#include <hip/hip_runtime.h>

__global__ __launch_bounds__(256, 1) void fused_block_kernel(const void* x, const void* wqkv, const void* wo, const void* wg,
                                                             const void* wu, const void* wd, void* out, void* ws) {
  asm volatile(
".Lstart_%=:\n"
"s_load_dwordx2 s[4:5], %0, 0\n"
"s_load_dwordx2 s[16:17], %0, 8\n"
"s_load_dwordx2 s[8:9], %0, 56\n"
"s_getpc_b64 s[24:25]\n"
"v_lshrrev_b32 v5, 6, %2\n"
"v_and_b32 v1, 63, %2\n"
"s_and_b32 s24, s24, 0xffffff80\n"
"s_mov_b32 s26, .Lend_%=-.Lstart_%=\n"
"s_mov_b32 s27, 0x20000\n"
"v_readfirstlane_b32 s15, v5\n"
"s_lshr_b32 s36, %1, 3\n"
"s_and_b32 s37, %1, 7\n"
"v_lshrrev_b32 v7, 2, v1\n"
"v_and_b32 v6, 3, v1\n"
"v_lshlrev_b32 v6, 7, v6\n"
"v_lshl_add_u32 v6, v7, 24, v6\n"
"s_lshl_b32 s40, s36, 2\n"
"s_add_u32 s40, s40, s15\n"
"s_lshl_b32 s40, s40, 9\n"
"s_mov_b32 m0, 163328\n"
"v_add_u32 v6, s40, v6\n"
"buffer_load_dword v6, s[24:27], 0 offen lds\n"
"s_mov_b32 s6, 2097152\n"
"s_mov_b32 s7, 0x20000\n"
"s_mov_b32 s18, 1572864\n"
"s_mov_b32 s19, 0x20000\n"
"s_lshr_b32 s40, s37, 1\n"
"s_lshr_b32 s41, s36, 2\n"
"s_lshl3_add_u32 s38, s40, s41\n"
"s_and_b32 s40, s37, 1\n"
"s_and_b32 s41, s36, 3\n"
"s_lshl2_add_u32 s39, s40, s41\n"
"v_lshrrev_b32 v5, 3, v1\n"
"v_and_b32 v6, 7, v1\n"
"v_lshrrev_b32 v7, 4, v1\n"
"v_xor_b32 v8, v6, v7\n"
"v_lshlrev_b32 v8, 4, v8\n"
"v_lshl_add_u32 v13, v5, 10, v8\n"
"v_xor_b32 v14, 64, v13\n"
"v_add_u32 v14, 0x1c00, v14\n"
"s_lshl_b32 s24, s15, 11\n"
"s_lshl_b32 s25, s15, 12\n"
"s_lshl1_add_u32 s26, s39, s38\n"
"s_and_b32 s26, s26, 7\n"
"s_lshl2_add_u32 s42, s38, s15\n"
"s_lshl_b32 s42, s42, 14\n"
"s_mul_i32 s40, s15, 49152\n"
"s_lshl_b32 s41, s39, 7\n"
"s_add_u32 s43, s40, s41\n"
"s_mov_b32 s40, s26\n"
"s_lshl_b32 s41, s40, 7\n"
"s_add_u32 s52, s41, s42\n"
"s_mul_i32 s41, s40, 196608\n"
"s_add_u32 s44, s41, s43\n"
"v_and_b32 v7, 1, v7\n"
"v_lshlrev_b32 v7, 2, v7\n"
"v_xor_b32 v8, v6, v7\n"
"v_lshlrev_b32 v8, 4, v8\n"
"v_mul_u32_u24 v17, 3072, v5\n"
"v_add_u32 v17, v17, v8\n"
"v_add_u32 v15, 1024, v17\n"
"v_add_u32 v16, 24576, v17\n"
"v_add_u32 v18, 23552, v17\n"
"s_waitcnt lgkmcnt(0)\n"
"s_add_u32 m0, s24, 0\n"
"s_nop 0\n"
"buffer_load_dwordx4 v13, s[4:7], s52 offen lds\n"
"s_add_u32 s40, s26, 1\n"
"s_and_b32 s40, s40, 7\n"
"buffer_load_dwordx4 v14, s[4:7], s52 offen offset:1024 lds\n"
"s_lshl_b32 s41, s40, 7\n"
"s_add_u32 s53, s41, s42\n"
"s_add_u32 m0, s25, 65536\n"
"s_nop 0\n"
"buffer_load_dwordx4 v15, s[16:19], s44 offen lds\n"
"s_mul_i32 s41, s40, 196608\n"
"s_add_u32 s45, s41, s43\n"
"buffer_load_dwordx4 v16, s[16:19], s44 offen offset:1024 lds\n"
"buffer_load_dwordx4 v17, s[16:19], s44 offen offset:2048 lds\n"
"buffer_load_dwordx4 v18, s[16:19], s44 offen offset:3072 lds\n"
"s_load_dwordx2 s[12:13], s[8:9], 0\n"
"s_load_dwordx2 s[20:21], %0, 16\n"
"s_mov_b32 s22, 524288\n"
"s_mov_b32 s23, 0x20000\n"
"s_mov_b32 s10, 16777216\n"
"s_mov_b32 s11, 0x20000\n"
"s_add_u32 m0, s24, 8192\n"
"s_nop 0\n"
"buffer_load_dwordx4 v13, s[4:7], s53 offen lds\n"
"s_add_u32 s40, s26, 2\n"
"s_and_b32 s40, s40, 7\n"
"s_lshl_b32 s41, s40, 7\n"
"s_add_u32 s54, s41, s42\n"
"s_mul_i32 s41, s40, 196608\n"
"s_add_u32 s46, s41, s43\n"
"v_and_b32 v2, 31, v1\n"
"v_lshrrev_b32 v3, 5, v1\n"
"v_lshlrev_b32 v4, 4, v1\n"
"s_and_b32 s27, s15, 1\n"
"s_lshr_b32 s60, s15, 1\n"
"s_xor_b32 s61, s27, s60\n"
"buffer_load_dwordx4 v14, s[4:7], s53 offen offset:1024 lds\n"
"v_bfe_u32 v6, v1, 1, 3\n"
"v_xor_b32 v6, v6, v3\n"
"v_lshlrev_b32 v6, 4, v6\n"
"v_lshl_add_u32 v6, v2, 7, v6\n"
"s_lshl_b32 s40, s27, 12\n"
"s_lshl_b32 s41, s61, 5\n"
"s_or_b32 s40, s40, s41\n"
"v_xor_b32 v20, s40, v6\n"
"v_xor_b32 v21, 32, v20\n"
"v_xor_b32 v22, 64, v20\n"
"v_xor_b32 v23, 96, v20\n"
"v_bfe_u32 v5, v1, 2, 2\n"
"s_add_u32 m0, s25, 81920\n"
"s_nop 0\n"
"buffer_load_dwordx4 v15, s[16:19], s45 offen lds\n"
"v_bfe_u32 v7, v1, 4, 1\n"
"v_and_b32 v8, 3, v1\n"
"v_lshlrev_b32 v8, 3, v8\n"
"v_lshl_add_u32 v19, v7, 5, v8\n"
"v_lshl_add_u32 v6, v3, 3, v5\n"
"v_lshl_add_u32 v6, v6, 7, v19\n"
"v_lshrrev_b32 v8, 1, v5\n"
"v_xor_b32 v8, s60, v8\n"
"v_lshl_add_u32 v6, v8, 6, v6\n"
"s_lshl_b32 s40, s61, 12\n"
"s_add_u32 s41, s40, 65536\n"
"s_sub_u32 s40, 65536, s40\n"
"buffer_load_dwordx4 v16, s[16:19], s45 offen offset:1024 lds\n"
"v_add_u32 v24, s41, v6\n"
"v_add_u32 v25, s40, v6\n"
"v_add_u32 v26, 0xc000, v24\n"
"v_add_u32 v27, 0xc000, v25\n"
"s_lshl_b32 s40, s27, 4\n"
"v_xor_b32 v28, s40, v24\n"
"v_xor_b32 v29, s40, v25\n"
"v_xor_b32 v30, s40, v26\n"
"v_xor_b32 v31, s40, v27\n"
"v_mov_b32 v32, 0x3f803f80\n"
"v_mov_b32 v33, 0x3f803f80\n"
"v_mov_b32 v34, 0x3f803f80\n"
"buffer_load_dwordx4 v17, s[16:19], s45 offen offset:2048 lds\n"
"v_mov_b32 v35, 0x3f803f80\n"
"s_add_u32 s40, s26, 3\n"
"s_and_b32 s40, s40, 7\n"
"s_lshl_b32 s41, s40, 7\n"
"s_add_u32 s55, s41, s42\n"
"s_mul_i32 s41, s40, 196608\n"
"s_add_u32 s47, s41, s43\n"
"s_add_u32 s40, s26, 4\n"
"s_and_b32 s40, s40, 7\n"
"s_lshl_b32 s41, s40, 7\n"
"s_add_u32 s56, s41, s42\n"
"s_mul_i32 s41, s40, 196608\n"
"buffer_load_dwordx4 v18, s[16:19], s45 offen offset:3072 lds\n"
"s_add_u32 s48, s41, s43\n"
"s_add_u32 s40, s26, 5\n"
"s_and_b32 s40, s40, 7\n"
"s_lshl_b32 s41, s40, 7\n"
"s_add_u32 s57, s41, s42\n"
"s_mul_i32 s41, s40, 196608\n"
"s_add_u32 s49, s41, s43\n"
"s_add_u32 s40, s26, 6\n"
"s_and_b32 s40, s40, 7\n"
"s_lshl_b32 s41, s40, 7\n"
"s_add_u32 s58, s41, s42\n"
"s_mul_i32 s41, s40, 196608\n"
"s_add_u32 m0, s24, 16384\n"
"s_nop 0\n"
"buffer_load_dwordx4 v13, s[4:7], s54 offen lds\n"
"s_add_u32 s50, s41, s43\n"
"s_add_u32 s40, s26, 7\n"
"s_and_b32 s40, s40, 7\n"
"s_lshl_b32 s41, s40, 7\n"
"s_add_u32 s59, s41, s42\n"
"s_mul_i32 s41, s40, 196608\n"
"s_add_u32 s51, s41, s43\n"
"s_lshr_b32 s40, s39, 2\n"
"s_lshl1_add_u32 s62, s38, s40\n"
"s_and_b32 s63, s39, 3\n"
"s_and_b32 s64, s38, 7\n"
"s_lshl_b32 s41, s40, 12\n"
"buffer_load_dwordx4 v14, s[4:7], s54 offen offset:1024 lds\n"
"s_lshl1_add_u32 s40, s63, s60\n"
"s_sub_u32 s40, s40, s26\n"
"s_and_b32 s40, s40, 7\n"
"s_lshl_b32 s40, s40, 13\n"
"s_add_u32 s40, s40, s41\n"
"v_bfe_u32 v5, v1, 1, 3\n"
"s_lshl_b32 s41, s27, 2\n"
"v_xor_b32 v5, s41, v5\n"
"v_lshlrev_b32 v5, 4, v5\n"
"v_lshl_add_u32 v5, v2, 7, v5\n"
"v_lshl_add_u32 v5, v3, 3, v5\n"
"v_add_u32 v36, s40, v5\n"
"s_add_u32 m0, s25, 98304\n"
"s_nop 0\n"
"buffer_load_dwordx4 v15, s[16:19], s46 offen lds\n"
"v_xor_b32 v37, 16, v36\n"
"v_xor_b32 v38, 32, v36\n"
"v_xor_b32 v39, 48, v36\n"
"s_lshl_b32 s40, s15, 10\n"
"s_add_u32 s40, s40, 147456\n"
"v_add_u32 v40, s40, v4\n"
"v_xor_b32 v41, 2048, v40\n"
"v_xor_b32 v42, 1024, v40\n"
"v_xor_b32 v43, 1152, v40\n"
"s_lshl_b32 s40, s15, 7\n"
"s_add_u32 s40, s40, 151552\n"
"v_lshl_add_u32 v44, v2, 2, s40\n"
"buffer_load_dwordx4 v16, s[16:19], s46 offen offset:1024 lds\n"
"v_lshl_add_u32 v45, v3, 4, s40\n"
"s_add_u32 s40, s24, 152576\n"
"v_add_u32 v46, s40, v4\n"
"s_xor_b32 s40, s24, 2048\n"
"s_add_u32 s40, s40, 152576\n"
"v_add_u32 v47, s40, v4\n"
"s_lshl_b32 s40, s15, 8\n"
"s_add_u32 s40, s40, 160768\n"
"v_lshl_add_u32 v48, v1, 2, s40\n"
"v_xor_b32 v49, 256, v48\n"
"v_mov_b32 v12, 2147483648\n"
"v_bfe_u32 v5, v1, 4, 1\n"
"buffer_load_dwordx4 v17, s[16:19], s46 offen offset:2048 lds\n"
"v_lshlrev_b32 v5, 3, v5\n"
"v_lshl_add_u32 v5, v3, 2, v5\n"
"v_bfe_u32 v6, v1, 3, 1\n"
"v_add_u32 v6, v5, v6\n"
"v_and_b32 v7, 0x70, v4\n"
"v_lshl_add_u32 v50, v6, 13, v7\n"
"v_and_b32 v7, 0xf0, v4\n"
"v_lshl_add_u32 v51, v5, 11, v7\n"
"v_cmp_gt_u32 vcc, 32, v7\n"
"v_lshrrev_b32 v56, 1, v7\n"
"v_add_u32 v56, 161792, v56\n"
"v_cndmask_b32 v51, v12, v51, vcc\n"
"buffer_load_dwordx4 v18, s[16:19], s46 offen offset:3072 lds\n"
"s_lshl_b32 s40, s15, 7\n"
"v_add_u32 v57, s40, v56\n"
"s_lshl_b32 s40, s39, 5\n"
"s_lshl4_add_u32 s41, s60, s40\n"
"s_lshl1_add_u32 s41, s27, s41\n"
"s_lshl_b32 s42, s41, 5\n"
"s_add_u32 s42, s42, s38\n"
"s_lshl_b32 s42, s42, 8\n"
"s_add_u32 s65, s42, 1048576\n"
"s_lshl4_add_u32 s42, s41, s38\n"
"s_lshl_b32 s42, s42, 7\n"
"s_add_u32 s66, s42, 3145728\n"
"s_add_u32 s40, s40, s38\n"
"s_lshl_b32 s42, s40, 13\n"
"s_add_u32 s42, s42, s24\n"
"s_add_u32 s67, s42, 1048576\n"
"s_lshl_b32 s42, s40, 11\n"
"s_add_u32 s68, s42, 3145728\n"
"s_lshl_b32 s42, s40, 7\n"
"s_add_u32 s71, s42, 528384\n"
"s_lshl_b32 s42, s40, 6\n"
"s_add_u32 s72, s42, 561152\n"
"s_and_b32 s43, s38, 1\n"
"s_or_b32 s43, s43, s15\n"
"s_cmp_eq_u32 s43, 0\n"
"s_cselect_b32 s42, 0, 2147483648\n"
"s_cselect_b64 s[74:75], -1, 0\n"
"v_lshrrev_b32 v7, 1, v1\n"
"v_and_b32 v8, 16, v4\n"
"v_lshl_add_u32 v52, v7, 7, v8\n"
"v_or_b32 v52, s42, v52\n"
"v_cmp_eq_u32 vcc, 0, v1\n"
"s_cmp_eq_u32 s15, 0\n"
"s_cselect_b32 s43, 0, 2147483648\n"
"v_cndmask_b32 v53, v12, v1, vcc\n"
"v_or_b32 v55, s42, v53\n"
"v_cmp_gt_u32 vcc, 8, v1\n"
"s_lshl1_add_u32 s40, s38, s27\n"
"s_lshl_b32 s40, s40, 4\n"
"v_cndmask_b32 v54, v12, v4, vcc\n"
"v_or_b32 v54, s43, v54\n"
"s_lshl1_add_u32 s40, s39, s40\n"
"s_add_u32 s40, s40, s60\n"
"s_lshl_b32 s40, s40, 11\n"
"s_add_u32 s69, s40, 4194304\n"
"s_lshl3_add_u32 s42, s38, s39\n"
"s_lshl2_add_u32 s42, s42, s15\n"
"s_lshl_b32 s42, s42, 7\n"
"s_add_u32 s70, s42, 4096\n"
"v_lshrrev_b32 v5, 4, v1\n"
"v_and_b32 v6, 0xf0, v4\n"
"v_lshlrev_b32 v7, 6, v5\n"
"v_xor_b32 v6, v6, v7\n"
"v_lshl_add_u32 v58, v5, 10, v6\n"
"v_add_u32 v59, 3072, v58\n"
"v_add_u32 v60, 6144, v58\n"
"v_add_u32 v61, 9216, v58\n"
"s_lshl2_add_u32 s40, s64, s15\n"
"s_lshl_b32 s40, s40, 14\n"
"s_lshl_b32 s41, s63, 8\n"
"s_add_u32 s73, s40, s41\n"
"v_mov_b32 v62, 897988541\n"
"v_mov_b32 v63, 989855744\n"
"s_mov_b32 s34, 1069066811\n"
"s_mov_b32 s35, 1069066811\n"
"s_mov_b32 s28, 0xaaaaaaaa\n"
"s_mov_b32 s29, 0xaaaaaaaa\n"
"s_mov_b32 s30, 0xcccccccc\n"
"s_mov_b32 s31, 0xcccccccc\n"
"s_mov_b32 s32, 0xffff0000\n"
"s_mov_b32 s33, 0xffff0000\n"
"s_waitcnt lgkmcnt(0)\n"
"s_add_u32 s12, s12, 1\n"
"s_addc_u32 s13, s13, 0\n"
"v_mov_b32 v66, s12\n"
"v_mov_b32 v67, s13\n"
"v_mov_b32 v70, s12\n"
"v_mov_b32 v71, s13\n"
"v_mov_b32 v74, s12\n"
"v_mov_b32 v75, s13\n"
"v_mov_b32 v76, s12\n"
"v_mov_b32 v77, s13\n"
"s_waitcnt vmcnt(12)\n"
"s_waitcnt lgkmcnt(0)\n"
"s_barrier\n"
"ds_read_b128 a[128:131], v20 offset:0\n"
"ds_read_b64_tr_b16 v[166:167], v28 offset:0\n"
"ds_read_b64_tr_b16 v[168:169], v28 offset:512\n"
"ds_read_b64_tr_b16 v[178:179], v24 offset:2048\n"
"ds_read_b64_tr_b16 v[180:181], v24 offset:2560\n"
"ds_read_b128 a[132:135], v21 offset:0\n"
"ds_read_b64_tr_b16 v[170:171], v29 offset:4096\n"
"ds_read_b64_tr_b16 v[172:173], v29 offset:4608\n"
"ds_read_b64_tr_b16 v[182:183], v25 offset:6144\n"
"ds_read_b64_tr_b16 v[184:185], v25 offset:6656\n"
"s_waitcnt lgkmcnt(7)\n"
"v_mfma_f32_32x32x16_bf16 v[78:93], a[128:131], v[166:169], 0\n"
"ds_read_b128 a[136:139], v22 offset:0\n"
"ds_read_b64_tr_b16 v[174:175], v28 offset:8192\n"
"ds_read_b64_tr_b16 v[176:177], v28 offset:8704\n"
"s_waitcnt lgkmcnt(8)\n"
"v_mfma_f32_32x32x16_bf16 v[94:109], a[128:131], v[178:181], 0\n"
"ds_read_b64_tr_b16 v[186:187], v24 offset:10240\n"
"ds_read_b64_tr_b16 v[188:189], v24 offset:10752\n"
"s_add_u32 m0, s24, 24576\n"
"s_nop 0\n"
"buffer_load_dwordx4 v13, s[4:7], s55 offen lds\n"
"v_mfma_f32_4x4x4_16b_bf16 v[126:129], v[32:33], a[128:129], 0\n"
"v_mfma_f32_4x4x4_16b_bf16 v[134:137], a[128:129], a[128:129], 0\n"
"v_mfma_f32_4x4x4_16b_bf16 v[130:133], v[32:33], a[130:131], 0\n"
"v_mfma_f32_4x4x4_16b_bf16 v[138:141], a[130:131], a[130:131], 0\n"
"s_waitcnt lgkmcnt(7)\n"
"v_mfma_f32_32x32x16_bf16 v[78:93], a[132:135], v[170:173], v[78:93]\n"
"ds_read_b128 a[140:143], v23 offset:0\n"
"ds_read_b64_tr_b16 v[166:167], v29 offset:12288\n"
"ds_read_b64_tr_b16 v[168:169], v29 offset:12800\n"
"buffer_load_dwordx4 v14, s[4:7], s55 offen offset:1024 lds\n"
"s_waitcnt lgkmcnt(8)\n"
"v_mfma_f32_32x32x16_bf16 v[94:109], a[132:135], v[182:185], v[94:109]\n"
"ds_read_b64_tr_b16 v[178:179], v25 offset:14336\n"
"ds_read_b64_tr_b16 v[180:181], v25 offset:14848\n"
"s_add_u32 m0, s25, 114688\n"
"s_nop 0\n"
"buffer_load_dwordx4 v15, s[16:19], s47 offen lds\n"
"v_mfma_f32_4x4x4_16b_bf16 v[142:145], v[32:33], v[170:171], 0\n"
"v_mfma_f32_4x4x4_16b_bf16 v[150:153], v[32:33], v[182:183], 0\n"
"v_mfma_f32_4x4x4_16b_bf16 v[146:149], v[32:33], v[172:173], 0\n"
"v_mfma_f32_4x4x4_16b_bf16 v[154:157], v[32:33], v[184:185], 0\n"
"s_waitcnt lgkmcnt(7)\n"
"v_mfma_f32_32x32x16_bf16 v[78:93], a[136:139], v[174:177], v[78:93]\n"
"s_waitcnt vmcnt(9)\n"
"s_waitcnt lgkmcnt(0)\n"
"s_barrier\n"
"ds_read_b128 a[144:147], v20 offset:8192\n"
"ds_read_b64_tr_b16 v[170:171], v28 offset:16384\n"
"ds_read_b64_tr_b16 v[172:173], v28 offset:16896\n"
"v_mfma_f32_32x32x16_bf16 v[94:109], a[136:139], v[186:189], v[94:109]\n"
"ds_read_b64_tr_b16 v[182:183], v24 offset:18432\n"
"ds_read_b64_tr_b16 v[184:185], v24 offset:18944\n"
"buffer_load_dwordx4 v16, s[16:19], s47 offen offset:1024 lds\n"
"v_mfma_f32_4x4x4_16b_bf16 v[126:129], v[32:33], a[136:137], v[126:129]\n"
"v_mfma_f32_4x4x4_16b_bf16 v[134:137], a[136:137], a[136:137], v[134:137]\n"
"v_mfma_f32_4x4x4_16b_bf16 v[130:133], v[32:33], a[138:139], v[130:133]\n"
"v_mfma_f32_4x4x4_16b_bf16 v[138:141], a[138:139], a[138:139], v[138:141]\n"
"v_mfma_f32_32x32x16_bf16 v[78:93], a[140:143], v[166:169], v[78:93]\n"
"ds_read_b128 a[148:151], v21 offset:8192\n"
"ds_read_b64_tr_b16 v[174:175], v29 offset:20480\n"
"ds_read_b64_tr_b16 v[176:177], v29 offset:20992\n"
"buffer_load_dwordx4 v17, s[16:19], s47 offen offset:2048 lds\n"
"v_mfma_f32_32x32x16_bf16 v[94:109], a[140:143], v[178:181], v[94:109]\n"
"ds_read_b64_tr_b16 v[186:187], v25 offset:22528\n"
"ds_read_b64_tr_b16 v[188:189], v25 offset:23040\n"
"buffer_load_dwordx4 v18, s[16:19], s47 offen offset:3072 lds\n"
"v_mfma_f32_4x4x4_16b_bf16 v[142:145], v[32:33], v[166:167], v[142:145]\n"
"v_mfma_f32_4x4x4_16b_bf16 v[150:153], v[32:33], v[178:179], v[150:153]\n"
"v_mfma_f32_4x4x4_16b_bf16 v[146:149], v[32:33], v[168:169], v[146:149]\n"
"v_mfma_f32_4x4x4_16b_bf16 v[154:157], v[32:33], v[180:181], v[154:157]\n"
"s_waitcnt lgkmcnt(7)\n"
"v_mfma_f32_32x32x16_bf16 v[78:93], a[144:147], v[170:173], v[78:93]\n"
"ds_read_b128 a[152:155], v22 offset:8192\n"
"ds_read_b64_tr_b16 v[166:167], v28 offset:24576\n"
"ds_read_b64_tr_b16 v[168:169], v28 offset:25088\n"
"s_waitcnt lgkmcnt(8)\n"
"v_mfma_f32_32x32x16_bf16 v[94:109], a[144:147], v[182:185], v[94:109]\n"
"ds_read_b64_tr_b16 v[178:179], v24 offset:26624\n"
"ds_read_b64_tr_b16 v[180:181], v24 offset:27136\n"
"s_add_u32 m0, s24, 32768\n"
"s_nop 0\n"
"buffer_load_dwordx4 v13, s[4:7], s56 offen lds\n"
"v_mfma_f32_4x4x4_16b_bf16 v[126:129], v[32:33], a[144:145], v[126:129]\n"
"v_mfma_f32_4x4x4_16b_bf16 v[134:137], a[144:145], a[144:145], v[134:137]\n"
"v_mfma_f32_4x4x4_16b_bf16 v[130:133], v[32:33], a[146:147], v[130:133]\n"
"v_mfma_f32_4x4x4_16b_bf16 v[138:141], a[146:147], a[146:147], v[138:141]\n"
"s_waitcnt lgkmcnt(7)\n"
"v_mfma_f32_32x32x16_bf16 v[78:93], a[148:151], v[174:177], v[78:93]\n"
"ds_read_b128 a[156:159], v23 offset:8192\n"
"ds_read_b64_tr_b16 v[170:171], v29 offset:28672\n"
"ds_read_b64_tr_b16 v[172:173], v29 offset:29184\n"
"buffer_load_dwordx4 v14, s[4:7], s56 offen offset:1024 lds\n"
"s_waitcnt lgkmcnt(8)\n"
"v_mfma_f32_32x32x16_bf16 v[94:109], a[148:151], v[186:189], v[94:109]\n"
"ds_read_b64_tr_b16 v[182:183], v25 offset:30720\n"
"ds_read_b64_tr_b16 v[184:185], v25 offset:31232\n"
"s_add_u32 m0, s25, 131072\n"
"s_nop 0\n"
"buffer_load_dwordx4 v15, s[16:19], s48 offen lds\n"
"v_mfma_f32_4x4x4_16b_bf16 v[142:145], v[32:33], v[174:175], v[142:145]\n"
"v_mfma_f32_4x4x4_16b_bf16 v[150:153], v[32:33], v[186:187], v[150:153]\n"
"v_mfma_f32_4x4x4_16b_bf16 v[146:149], v[32:33], v[176:177], v[146:149]\n"
"v_mfma_f32_4x4x4_16b_bf16 v[154:157], v[32:33], v[188:189], v[154:157]\n"
"s_waitcnt lgkmcnt(7)\n"
"v_mfma_f32_32x32x16_bf16 v[78:93], a[152:155], v[166:169], v[78:93]\n"
"s_waitcnt vmcnt(9)\n"
"s_waitcnt lgkmcnt(0)\n"
"s_barrier\n"
"ds_read_b128 a[160:163], v20 offset:16384\n"
"ds_read_b64_tr_b16 v[174:175], v28 offset:32768\n"
"ds_read_b64_tr_b16 v[176:177], v28 offset:33280\n"
"v_mfma_f32_32x32x16_bf16 v[94:109], a[152:155], v[178:181], v[94:109]\n"
"ds_read_b64_tr_b16 v[186:187], v24 offset:34816\n"
"ds_read_b64_tr_b16 v[188:189], v24 offset:35328\n"
"buffer_load_dwordx4 v16, s[16:19], s48 offen offset:1024 lds\n"
"v_mfma_f32_4x4x4_16b_bf16 v[126:129], v[32:33], a[152:153], v[126:129]\n"
"v_mfma_f32_4x4x4_16b_bf16 v[134:137], a[152:153], a[152:153], v[134:137]\n"
"v_mfma_f32_4x4x4_16b_bf16 v[130:133], v[32:33], a[154:155], v[130:133]\n"
"v_mfma_f32_4x4x4_16b_bf16 v[138:141], a[154:155], a[154:155], v[138:141]\n"
"v_mfma_f32_32x32x16_bf16 v[78:93], a[156:159], v[170:173], v[78:93]\n"
"ds_read_b128 a[164:167], v21 offset:16384\n"
"ds_read_b64_tr_b16 v[166:167], v29 offset:36864\n"
"ds_read_b64_tr_b16 v[168:169], v29 offset:37376\n"
"buffer_load_dwordx4 v17, s[16:19], s48 offen offset:2048 lds\n"
"v_mfma_f32_32x32x16_bf16 v[94:109], a[156:159], v[182:185], v[94:109]\n"
"ds_read_b64_tr_b16 v[178:179], v25 offset:38912\n"
"ds_read_b64_tr_b16 v[180:181], v25 offset:39424\n"
"buffer_load_dwordx4 v18, s[16:19], s48 offen offset:3072 lds\n"
"v_mfma_f32_4x4x4_16b_bf16 v[142:145], v[32:33], v[170:171], v[142:145]\n"
"v_mfma_f32_4x4x4_16b_bf16 v[150:153], v[32:33], v[182:183], v[150:153]\n"
"v_mfma_f32_4x4x4_16b_bf16 v[146:149], v[32:33], v[172:173], v[146:149]\n"
"v_mfma_f32_4x4x4_16b_bf16 v[154:157], v[32:33], v[184:185], v[154:157]\n"
"s_waitcnt lgkmcnt(7)\n"
"v_mfma_f32_32x32x16_bf16 v[78:93], a[160:163], v[174:177], v[78:93]\n"
"ds_read_b128 a[168:171], v22 offset:16384\n"
"ds_read_b64_tr_b16 v[170:171], v28 offset:40960\n"
"ds_read_b64_tr_b16 v[172:173], v28 offset:41472\n"
"s_waitcnt lgkmcnt(8)\n"
"v_mfma_f32_32x32x16_bf16 v[94:109], a[160:163], v[186:189], v[94:109]\n"
"ds_read_b64_tr_b16 v[182:183], v24 offset:43008\n"
"ds_read_b64_tr_b16 v[184:185], v24 offset:43520\n"
"s_add_u32 m0, s24, 40960\n"
"s_nop 0\n"
"buffer_load_dwordx4 v13, s[4:7], s57 offen lds\n"
"v_mfma_f32_4x4x4_16b_bf16 v[126:129], v[32:33], a[160:161], v[126:129]\n"
"v_mfma_f32_4x4x4_16b_bf16 v[134:137], a[160:161], a[160:161], v[134:137]\n"
"v_mfma_f32_4x4x4_16b_bf16 v[130:133], v[32:33], a[162:163], v[130:133]\n"
"v_mfma_f32_4x4x4_16b_bf16 v[138:141], a[162:163], a[162:163], v[138:141]\n"
"s_waitcnt lgkmcnt(7)\n"
"v_mfma_f32_32x32x16_bf16 v[78:93], a[164:167], v[166:169], v[78:93]\n"
"ds_read_b128 a[172:175], v23 offset:16384\n"
"ds_read_b64_tr_b16 v[174:175], v29 offset:45056\n"
"ds_read_b64_tr_b16 v[176:177], v29 offset:45568\n"
"buffer_load_dwordx4 v14, s[4:7], s57 offen offset:1024 lds\n"
"s_waitcnt lgkmcnt(8)\n"
"v_mfma_f32_32x32x16_bf16 v[94:109], a[164:167], v[178:181], v[94:109]\n"
"ds_read_b64_tr_b16 v[186:187], v25 offset:47104\n"
"ds_read_b64_tr_b16 v[188:189], v25 offset:47616\n"
"s_add_u32 m0, s25, 65536\n"
"s_nop 0\n"
"buffer_load_dwordx4 v15, s[16:19], s49 offen lds\n"
"v_mfma_f32_4x4x4_16b_bf16 v[142:145], v[32:33], v[166:167], v[142:145]\n"
"v_mfma_f32_4x4x4_16b_bf16 v[150:153], v[32:33], v[178:179], v[150:153]\n"
"v_mfma_f32_4x4x4_16b_bf16 v[146:149], v[32:33], v[168:169], v[146:149]\n"
"v_mfma_f32_4x4x4_16b_bf16 v[154:157], v[32:33], v[180:181], v[154:157]\n"
"s_waitcnt lgkmcnt(7)\n"
"v_mfma_f32_32x32x16_bf16 v[78:93], a[168:171], v[170:173], v[78:93]\n"
"s_waitcnt vmcnt(9)\n"
"s_waitcnt lgkmcnt(0)\n"
"s_barrier\n"
"ds_read_b128 a[176:179], v20 offset:24576\n"
"ds_read_b64_tr_b16 v[166:167], v30 offset:0\n"
"ds_read_b64_tr_b16 v[168:169], v30 offset:512\n"
"v_mfma_f32_32x32x16_bf16 v[94:109], a[168:171], v[182:185], v[94:109]\n"
"ds_read_b64_tr_b16 v[178:179], v26 offset:2048\n"
"ds_read_b64_tr_b16 v[180:181], v26 offset:2560\n"
"buffer_load_dwordx4 v16, s[16:19], s49 offen offset:1024 lds\n"
"v_mfma_f32_4x4x4_16b_bf16 v[126:129], v[32:33], a[168:169], v[126:129]\n"
"v_mfma_f32_4x4x4_16b_bf16 v[134:137], a[168:169], a[168:169], v[134:137]\n"
"v_mfma_f32_4x4x4_16b_bf16 v[130:133], v[32:33], a[170:171], v[130:133]\n"
"v_mfma_f32_4x4x4_16b_bf16 v[138:141], a[170:171], a[170:171], v[138:141]\n"
"v_mfma_f32_32x32x16_bf16 v[78:93], a[172:175], v[174:177], v[78:93]\n"
"ds_read_b128 a[180:183], v21 offset:24576\n"
"ds_read_b64_tr_b16 v[170:171], v31 offset:4096\n"
"ds_read_b64_tr_b16 v[172:173], v31 offset:4608\n"
"buffer_load_dwordx4 v17, s[16:19], s49 offen offset:2048 lds\n"
"v_mfma_f32_32x32x16_bf16 v[94:109], a[172:175], v[186:189], v[94:109]\n"
"ds_read_b64_tr_b16 v[182:183], v27 offset:6144\n"
"ds_read_b64_tr_b16 v[184:185], v27 offset:6656\n"
"buffer_load_dwordx4 v18, s[16:19], s49 offen offset:3072 lds\n"
"v_mfma_f32_4x4x4_16b_bf16 v[142:145], v[32:33], v[174:175], v[142:145]\n"
"v_mfma_f32_4x4x4_16b_bf16 v[150:153], v[32:33], v[186:187], v[150:153]\n"
"v_mfma_f32_4x4x4_16b_bf16 v[146:149], v[32:33], v[176:177], v[146:149]\n"
"v_mfma_f32_4x4x4_16b_bf16 v[154:157], v[32:33], v[188:189], v[154:157]\n"
"s_waitcnt lgkmcnt(7)\n"
"v_mfma_f32_32x32x16_bf16 v[78:93], a[176:179], v[166:169], v[78:93]\n"
"ds_read_b128 a[184:187], v22 offset:24576\n"
"ds_read_b64_tr_b16 v[174:175], v30 offset:8192\n"
"ds_read_b64_tr_b16 v[176:177], v30 offset:8704\n"
"s_waitcnt lgkmcnt(8)\n"
"v_mfma_f32_32x32x16_bf16 v[94:109], a[176:179], v[178:181], v[94:109]\n"
"ds_read_b64_tr_b16 v[186:187], v26 offset:10240\n"
"ds_read_b64_tr_b16 v[188:189], v26 offset:10752\n"
"s_add_u32 m0, s24, 49152\n"
"s_nop 0\n"
"buffer_load_dwordx4 v13, s[4:7], s58 offen lds\n"
"v_mfma_f32_4x4x4_16b_bf16 v[126:129], v[32:33], a[176:177], v[126:129]\n"
"v_mfma_f32_4x4x4_16b_bf16 v[134:137], a[176:177], a[176:177], v[134:137]\n"
"v_mfma_f32_4x4x4_16b_bf16 v[130:133], v[32:33], a[178:179], v[130:133]\n"
"v_mfma_f32_4x4x4_16b_bf16 v[138:141], a[178:179], a[178:179], v[138:141]\n"
"s_waitcnt lgkmcnt(7)\n"
"v_mfma_f32_32x32x16_bf16 v[78:93], a[180:183], v[170:173], v[78:93]\n"
"ds_read_b128 a[188:191], v23 offset:24576\n"
"ds_read_b64_tr_b16 v[166:167], v31 offset:12288\n"
"ds_read_b64_tr_b16 v[168:169], v31 offset:12800\n"
"buffer_load_dwordx4 v14, s[4:7], s58 offen offset:1024 lds\n"
"s_waitcnt lgkmcnt(8)\n"
"v_mfma_f32_32x32x16_bf16 v[94:109], a[180:183], v[182:185], v[94:109]\n"
"ds_read_b64_tr_b16 v[178:179], v27 offset:14336\n"
"ds_read_b64_tr_b16 v[180:181], v27 offset:14848\n"
"s_add_u32 m0, s25, 81920\n"
"s_nop 0\n"
"buffer_load_dwordx4 v15, s[16:19], s50 offen lds\n"
"v_mfma_f32_4x4x4_16b_bf16 v[142:145], v[32:33], v[170:171], v[142:145]\n"
"v_mfma_f32_4x4x4_16b_bf16 v[150:153], v[32:33], v[182:183], v[150:153]\n"
"v_mfma_f32_4x4x4_16b_bf16 v[146:149], v[32:33], v[172:173], v[146:149]\n"
"v_mfma_f32_4x4x4_16b_bf16 v[154:157], v[32:33], v[184:185], v[154:157]\n"
"s_waitcnt lgkmcnt(7)\n"
"v_mfma_f32_32x32x16_bf16 v[78:93], a[184:187], v[174:177], v[78:93]\n"
"s_waitcnt vmcnt(9)\n"
"s_waitcnt lgkmcnt(0)\n"
"s_barrier\n"
"ds_read_b128 a[192:195], v20 offset:32768\n"
"ds_read_b64_tr_b16 v[170:171], v30 offset:16384\n"
"ds_read_b64_tr_b16 v[172:173], v30 offset:16896\n"
"v_mfma_f32_32x32x16_bf16 v[94:109], a[184:187], v[186:189], v[94:109]\n"
"ds_read_b64_tr_b16 v[182:183], v26 offset:18432\n"
"ds_read_b64_tr_b16 v[184:185], v26 offset:18944\n"
"buffer_load_dwordx4 v16, s[16:19], s50 offen offset:1024 lds\n"
"v_mfma_f32_4x4x4_16b_bf16 v[126:129], v[32:33], a[184:185], v[126:129]\n"
"v_mfma_f32_4x4x4_16b_bf16 v[134:137], a[184:185], a[184:185], v[134:137]\n"
"v_mfma_f32_4x4x4_16b_bf16 v[130:133], v[32:33], a[186:187], v[130:133]\n"
"v_mfma_f32_4x4x4_16b_bf16 v[138:141], a[186:187], a[186:187], v[138:141]\n"
"v_mfma_f32_32x32x16_bf16 v[78:93], a[188:191], v[166:169], v[78:93]\n"
"ds_read_b128 a[196:199], v21 offset:32768\n"
"ds_read_b64_tr_b16 v[174:175], v31 offset:20480\n"
"ds_read_b64_tr_b16 v[176:177], v31 offset:20992\n"
"buffer_load_dwordx4 v17, s[16:19], s50 offen offset:2048 lds\n"
"v_mfma_f32_32x32x16_bf16 v[94:109], a[188:191], v[178:181], v[94:109]\n"
"ds_read_b64_tr_b16 v[186:187], v27 offset:22528\n"
"ds_read_b64_tr_b16 v[188:189], v27 offset:23040\n"
"buffer_load_dwordx4 v18, s[16:19], s50 offen offset:3072 lds\n"
"v_mfma_f32_4x4x4_16b_bf16 v[142:145], v[32:33], v[166:167], v[142:145]\n"
"v_mfma_f32_4x4x4_16b_bf16 v[150:153], v[32:33], v[178:179], v[150:153]\n"
"v_mfma_f32_4x4x4_16b_bf16 v[146:149], v[32:33], v[168:169], v[146:149]\n"
"v_mfma_f32_4x4x4_16b_bf16 v[154:157], v[32:33], v[180:181], v[154:157]\n"
"s_waitcnt lgkmcnt(7)\n"
"v_mfma_f32_32x32x16_bf16 v[78:93], a[192:195], v[170:173], v[78:93]\n"
"ds_read_b128 a[200:203], v22 offset:32768\n"
"ds_read_b64_tr_b16 v[166:167], v30 offset:24576\n"
"ds_read_b64_tr_b16 v[168:169], v30 offset:25088\n"
"s_waitcnt lgkmcnt(8)\n"
"v_mfma_f32_32x32x16_bf16 v[94:109], a[192:195], v[182:185], v[94:109]\n"
"ds_read_b64_tr_b16 v[178:179], v26 offset:26624\n"
"ds_read_b64_tr_b16 v[180:181], v26 offset:27136\n"
"s_add_u32 m0, s24, 57344\n"
"s_nop 0\n"
"buffer_load_dwordx4 v13, s[4:7], s59 offen lds\n"
"v_mfma_f32_4x4x4_16b_bf16 v[126:129], v[32:33], a[192:193], v[126:129]\n"
"v_mfma_f32_4x4x4_16b_bf16 v[134:137], a[192:193], a[192:193], v[134:137]\n"
"v_mfma_f32_4x4x4_16b_bf16 v[130:133], v[32:33], a[194:195], v[130:133]\n"
"v_mfma_f32_4x4x4_16b_bf16 v[138:141], a[194:195], a[194:195], v[138:141]\n"
"s_waitcnt lgkmcnt(7)\n"
"v_mfma_f32_32x32x16_bf16 v[78:93], a[196:199], v[174:177], v[78:93]\n"
"ds_read_b128 a[204:207], v23 offset:32768\n"
"ds_read_b64_tr_b16 v[170:171], v31 offset:28672\n"
"ds_read_b64_tr_b16 v[172:173], v31 offset:29184\n"
"buffer_load_dwordx4 v14, s[4:7], s59 offen offset:1024 lds\n"
"s_waitcnt lgkmcnt(8)\n"
"v_mfma_f32_32x32x16_bf16 v[94:109], a[196:199], v[186:189], v[94:109]\n"
"ds_read_b64_tr_b16 v[182:183], v27 offset:30720\n"
"ds_read_b64_tr_b16 v[184:185], v27 offset:31232\n"
"s_add_u32 m0, s25, 98304\n"
"s_nop 0\n"
"buffer_load_dwordx4 v15, s[16:19], s51 offen lds\n"
"v_mfma_f32_4x4x4_16b_bf16 v[142:145], v[32:33], v[174:175], v[142:145]\n"
"v_mfma_f32_4x4x4_16b_bf16 v[150:153], v[32:33], v[186:187], v[150:153]\n"
"v_mfma_f32_4x4x4_16b_bf16 v[146:149], v[32:33], v[176:177], v[146:149]\n"
"v_mfma_f32_4x4x4_16b_bf16 v[154:157], v[32:33], v[188:189], v[154:157]\n"
"s_waitcnt lgkmcnt(7)\n"
"v_mfma_f32_32x32x16_bf16 v[78:93], a[200:203], v[166:169], v[78:93]\n"
"s_waitcnt vmcnt(9)\n"
"s_waitcnt lgkmcnt(0)\n"
"s_barrier\n"
"ds_read_b128 a[208:211], v20 offset:40960\n"
"ds_read_b64_tr_b16 v[174:175], v28 offset:0\n"
"ds_read_b64_tr_b16 v[176:177], v28 offset:512\n"
"v_mfma_f32_32x32x16_bf16 v[94:109], a[200:203], v[178:181], v[94:109]\n"
"ds_read_b64_tr_b16 v[186:187], v24 offset:2048\n"
"ds_read_b64_tr_b16 v[188:189], v24 offset:2560\n"
"buffer_load_dwordx4 v16, s[16:19], s51 offen offset:1024 lds\n"
"v_mfma_f32_4x4x4_16b_bf16 v[126:129], v[32:33], a[200:201], v[126:129]\n"
"v_mfma_f32_4x4x4_16b_bf16 v[134:137], a[200:201], a[200:201], v[134:137]\n"
"v_mfma_f32_4x4x4_16b_bf16 v[130:133], v[32:33], a[202:203], v[130:133]\n"
"v_mfma_f32_4x4x4_16b_bf16 v[138:141], a[202:203], a[202:203], v[138:141]\n"
"v_mfma_f32_32x32x16_bf16 v[78:93], a[204:207], v[170:173], v[78:93]\n"
"ds_read_b128 a[212:215], v21 offset:40960\n"
"ds_read_b64_tr_b16 v[166:167], v29 offset:4096\n"
"ds_read_b64_tr_b16 v[168:169], v29 offset:4608\n"
"buffer_load_dwordx4 v17, s[16:19], s51 offen offset:2048 lds\n"
"v_mfma_f32_32x32x16_bf16 v[94:109], a[204:207], v[182:185], v[94:109]\n"
"ds_read_b64_tr_b16 v[178:179], v25 offset:6144\n"
"ds_read_b64_tr_b16 v[180:181], v25 offset:6656\n"
"buffer_load_dwordx4 v18, s[16:19], s51 offen offset:3072 lds\n"
"v_mfma_f32_4x4x4_16b_bf16 v[142:145], v[32:33], v[170:171], v[142:145]\n"
"v_mfma_f32_4x4x4_16b_bf16 v[150:153], v[32:33], v[182:183], v[150:153]\n"
"v_mfma_f32_4x4x4_16b_bf16 v[146:149], v[32:33], v[172:173], v[146:149]\n"
"v_mfma_f32_4x4x4_16b_bf16 v[154:157], v[32:33], v[184:185], v[154:157]\n"
"s_waitcnt lgkmcnt(7)\n"
"v_mfma_f32_32x32x16_bf16 v[78:93], a[208:211], v[174:177], v[78:93]\n"
"ds_read_b128 a[216:219], v22 offset:40960\n"
"ds_read_b64_tr_b16 v[170:171], v28 offset:8192\n"
"ds_read_b64_tr_b16 v[172:173], v28 offset:8704\n"
"s_waitcnt lgkmcnt(8)\n"
"v_mfma_f32_32x32x16_bf16 v[94:109], a[208:211], v[186:189], v[94:109]\n"
"ds_read_b64_tr_b16 v[182:183], v24 offset:10240\n"
"ds_read_b64_tr_b16 v[184:185], v24 offset:10752\n"
"s_add_u32 m0, s25, 114688\n"
"s_nop 0\n"
"buffer_load_dwordx4 v17, s[16:19], s44 offen lds\n"
"v_mfma_f32_4x4x4_16b_bf16 v[126:129], v[32:33], a[208:209], v[126:129]\n"
"v_mfma_f32_4x4x4_16b_bf16 v[134:137], a[208:209], a[208:209], v[134:137]\n"
"v_mfma_f32_4x4x4_16b_bf16 v[130:133], v[32:33], a[210:211], v[130:133]\n"
"v_mfma_f32_4x4x4_16b_bf16 v[138:141], a[210:211], a[210:211], v[138:141]\n"
"s_waitcnt lgkmcnt(7)\n"
"v_mfma_f32_32x32x16_bf16 v[78:93], a[212:215], v[166:169], v[78:93]\n"
"ds_read_b128 a[220:223], v23 offset:40960\n"
"ds_read_b64_tr_b16 v[174:175], v29 offset:12288\n"
"ds_read_b64_tr_b16 v[176:177], v29 offset:12800\n"
"buffer_load_dwordx4 v18, s[16:19], s44 offen offset:1024 lds\n"
"s_waitcnt lgkmcnt(8)\n"
"v_mfma_f32_32x32x16_bf16 v[94:109], a[212:215], v[178:181], v[94:109]\n"
"ds_read_b64_tr_b16 v[186:187], v25 offset:14336\n"
"ds_read_b64_tr_b16 v[188:189], v25 offset:14848\n"
"s_add_u32 m0, s25, 116736\n"
"s_nop 0\n"
"buffer_load_dwordx4 v17, s[16:19], s45 offen lds\n"
"v_mfma_f32_4x4x4_16b_bf16 v[142:145], v[32:33], v[166:167], v[142:145]\n"
"v_mfma_f32_4x4x4_16b_bf16 v[150:153], v[32:33], v[178:179], v[150:153]\n"
"v_mfma_f32_4x4x4_16b_bf16 v[146:149], v[32:33], v[168:169], v[146:149]\n"
"v_mfma_f32_4x4x4_16b_bf16 v[154:157], v[32:33], v[180:181], v[154:157]\n"
"s_waitcnt lgkmcnt(7)\n"
"v_mfma_f32_32x32x16_bf16 v[78:93], a[216:219], v[170:173], v[78:93]\n"
"s_waitcnt vmcnt(9)\n"
"s_waitcnt lgkmcnt(0)\n"
"s_barrier\n"
"ds_read_b128 a[224:227], v20 offset:49152\n"
"ds_read_b64_tr_b16 v[166:167], v28 offset:16384\n"
"ds_read_b64_tr_b16 v[168:169], v28 offset:16896\n"
"v_mfma_f32_32x32x16_bf16 v[94:109], a[216:219], v[182:185], v[94:109]\n"
"ds_read_b64_tr_b16 v[178:179], v24 offset:18432\n"
"ds_read_b64_tr_b16 v[180:181], v24 offset:18944\n"
"buffer_load_dwordx4 v18, s[16:19], s45 offen offset:1024 lds\n"
"v_mfma_f32_4x4x4_16b_bf16 v[126:129], v[32:33], a[216:217], v[126:129]\n"
"v_mfma_f32_4x4x4_16b_bf16 v[134:137], a[216:217], a[216:217], v[134:137]\n"
"v_mfma_f32_4x4x4_16b_bf16 v[130:133], v[32:33], a[218:219], v[130:133]\n"
"v_mfma_f32_4x4x4_16b_bf16 v[138:141], a[218:219], a[218:219], v[138:141]\n"
"v_mfma_f32_32x32x16_bf16 v[78:93], a[220:223], v[174:177], v[78:93]\n"
"ds_read_b128 a[228:231], v21 offset:49152\n"
"ds_read_b64_tr_b16 v[170:171], v29 offset:20480\n"
"ds_read_b64_tr_b16 v[172:173], v29 offset:20992\n"
"s_add_u32 m0, s25, 131072\n"
"s_nop 0\n"
"buffer_load_dwordx4 v17, s[16:19], s46 offen lds\n"
"v_mfma_f32_32x32x16_bf16 v[94:109], a[220:223], v[186:189], v[94:109]\n"
"ds_read_b64_tr_b16 v[182:183], v25 offset:22528\n"
"ds_read_b64_tr_b16 v[184:185], v25 offset:23040\n"
"buffer_load_dwordx4 v18, s[16:19], s46 offen offset:1024 lds\n"
"v_mfma_f32_4x4x4_16b_bf16 v[142:145], v[32:33], v[174:175], v[142:145]\n"
"v_mfma_f32_4x4x4_16b_bf16 v[150:153], v[32:33], v[186:187], v[150:153]\n"
"v_mfma_f32_4x4x4_16b_bf16 v[146:149], v[32:33], v[176:177], v[146:149]\n"
"v_mfma_f32_4x4x4_16b_bf16 v[154:157], v[32:33], v[188:189], v[154:157]\n"
"s_waitcnt lgkmcnt(7)\n"
"v_mfma_f32_32x32x16_bf16 v[78:93], a[224:227], v[166:169], v[78:93]\n"
"ds_read_b128 a[232:235], v22 offset:49152\n"
"ds_read_b64_tr_b16 v[174:175], v28 offset:24576\n"
"ds_read_b64_tr_b16 v[176:177], v28 offset:25088\n"
"s_waitcnt lgkmcnt(8)\n"
"v_mfma_f32_32x32x16_bf16 v[94:109], a[224:227], v[178:181], v[94:109]\n"
"ds_read_b64_tr_b16 v[186:187], v24 offset:26624\n"
"ds_read_b64_tr_b16 v[188:189], v24 offset:27136\n"
"s_add_u32 m0, s25, 133120\n"
"s_nop 0\n"
"buffer_load_dwordx4 v17, s[16:19], s47 offen lds\n"
"v_mfma_f32_4x4x4_16b_bf16 v[126:129], v[32:33], a[224:225], v[126:129]\n"
"v_mfma_f32_4x4x4_16b_bf16 v[134:137], a[224:225], a[224:225], v[134:137]\n"
"v_mfma_f32_4x4x4_16b_bf16 v[130:133], v[32:33], a[226:227], v[130:133]\n"
"v_mfma_f32_4x4x4_16b_bf16 v[138:141], a[226:227], a[226:227], v[138:141]\n"
"s_waitcnt lgkmcnt(7)\n"
"v_mfma_f32_32x32x16_bf16 v[78:93], a[228:231], v[170:173], v[78:93]\n"
"ds_read_b128 a[236:239], v23 offset:49152\n"
"ds_read_b64_tr_b16 v[166:167], v29 offset:28672\n"
"ds_read_b64_tr_b16 v[168:169], v29 offset:29184\n"
"buffer_load_dwordx4 v18, s[16:19], s47 offen offset:1024 lds\n"
"s_waitcnt lgkmcnt(8)\n"
"v_mfma_f32_32x32x16_bf16 v[94:109], a[228:231], v[182:185], v[94:109]\n"
"ds_read_b64_tr_b16 v[178:179], v25 offset:30720\n"
"ds_read_b64_tr_b16 v[180:181], v25 offset:31232\n"
"v_mfma_f32_4x4x4_16b_bf16 v[142:145], v[32:33], v[170:171], v[142:145]\n"
"v_mfma_f32_4x4x4_16b_bf16 v[150:153], v[32:33], v[182:183], v[150:153]\n"
"v_mfma_f32_4x4x4_16b_bf16 v[146:149], v[32:33], v[172:173], v[146:149]\n"
"v_mfma_f32_4x4x4_16b_bf16 v[154:157], v[32:33], v[184:185], v[154:157]\n"
"s_waitcnt lgkmcnt(7)\n"
"v_mfma_f32_32x32x16_bf16 v[78:93], a[232:235], v[174:177], v[78:93]\n"
"s_waitcnt vmcnt(8)\n"
"s_waitcnt lgkmcnt(0)\n"
"s_barrier\n"
"ds_read_b128 a[240:243], v20 offset:57344\n"
"ds_read_b64_tr_b16 v[170:171], v28 offset:32768\n"
"ds_read_b64_tr_b16 v[172:173], v28 offset:33280\n"
"s_add_u32 m0, s25, 65536\n"
"s_nop 0\n"
"buffer_load_dwordx4 v17, s[16:19], s48 offen lds\n"
"v_mfma_f32_32x32x16_bf16 v[94:109], a[232:235], v[186:189], v[94:109]\n"
"ds_read_b64_tr_b16 v[182:183], v24 offset:34816\n"
"ds_read_b64_tr_b16 v[184:185], v24 offset:35328\n"
"buffer_load_dwordx4 v18, s[16:19], s48 offen offset:1024 lds\n"
"v_mfma_f32_4x4x4_16b_bf16 v[126:129], v[32:33], a[232:233], v[126:129]\n"
"v_mfma_f32_4x4x4_16b_bf16 v[134:137], a[232:233], a[232:233], v[134:137]\n"
"v_mfma_f32_4x4x4_16b_bf16 v[130:133], v[32:33], a[234:235], v[130:133]\n"
"v_mfma_f32_4x4x4_16b_bf16 v[138:141], a[234:235], a[234:235], v[138:141]\n"
"v_mfma_f32_32x32x16_bf16 v[78:93], a[236:239], v[166:169], v[78:93]\n"
"ds_read_b128 a[244:247], v21 offset:57344\n"
"ds_read_b64_tr_b16 v[174:175], v29 offset:36864\n"
"ds_read_b64_tr_b16 v[176:177], v29 offset:37376\n"
"s_add_u32 m0, s25, 67584\n"
"s_nop 0\n"
"buffer_load_dwordx4 v17, s[16:19], s49 offen lds\n"
"v_mfma_f32_32x32x16_bf16 v[94:109], a[236:239], v[178:181], v[94:109]\n"
"ds_read_b64_tr_b16 v[186:187], v25 offset:38912\n"
"ds_read_b64_tr_b16 v[188:189], v25 offset:39424\n"
"buffer_load_dwordx4 v18, s[16:19], s49 offen offset:1024 lds\n"
"v_mfma_f32_4x4x4_16b_bf16 v[142:145], v[32:33], v[166:167], v[142:145]\n"
"v_mfma_f32_4x4x4_16b_bf16 v[150:153], v[32:33], v[178:179], v[150:153]\n"
"v_mfma_f32_4x4x4_16b_bf16 v[146:149], v[32:33], v[168:169], v[146:149]\n"
"v_mfma_f32_4x4x4_16b_bf16 v[154:157], v[32:33], v[180:181], v[154:157]\n"
"s_waitcnt lgkmcnt(7)\n"
"v_mfma_f32_32x32x16_bf16 v[78:93], a[240:243], v[170:173], v[78:93]\n"
"ds_read_b128 a[248:251], v22 offset:57344\n"
"ds_read_b64_tr_b16 v[166:167], v28 offset:40960\n"
"ds_read_b64_tr_b16 v[168:169], v28 offset:41472\n"
"s_add_u32 m0, s25, 81920\n"
"s_nop 0\n"
"buffer_load_dwordx4 v17, s[16:19], s50 offen lds\n"
"s_waitcnt lgkmcnt(8)\n"
"v_mfma_f32_32x32x16_bf16 v[94:109], a[240:243], v[182:185], v[94:109]\n"
"ds_read_b64_tr_b16 v[178:179], v24 offset:43008\n"
"ds_read_b64_tr_b16 v[180:181], v24 offset:43520\n"
"buffer_load_dwordx4 v18, s[16:19], s50 offen offset:1024 lds\n"
"v_mfma_f32_4x4x4_16b_bf16 v[126:129], v[32:33], a[240:241], v[126:129]\n"
"v_mfma_f32_4x4x4_16b_bf16 v[134:137], a[240:241], a[240:241], v[134:137]\n"
"v_mfma_f32_4x4x4_16b_bf16 v[130:133], v[32:33], a[242:243], v[130:133]\n"
"v_mfma_f32_4x4x4_16b_bf16 v[138:141], a[242:243], a[242:243], v[138:141]\n"
"s_waitcnt lgkmcnt(7)\n"
"v_mfma_f32_32x32x16_bf16 v[78:93], a[244:247], v[174:177], v[78:93]\n"
"ds_read_b128 a[252:255], v23 offset:57344\n"
"ds_read_b64_tr_b16 v[170:171], v29 offset:45056\n"
"ds_read_b64_tr_b16 v[172:173], v29 offset:45568\n"
"s_add_u32 m0, s25, 83968\n"
"s_nop 0\n"
"buffer_load_dwordx4 v17, s[16:19], s51 offen lds\n"
"s_waitcnt lgkmcnt(8)\n"
"v_mfma_f32_32x32x16_bf16 v[94:109], a[244:247], v[186:189], v[94:109]\n"
"ds_read_b64_tr_b16 v[182:183], v25 offset:47104\n"
"ds_read_b64_tr_b16 v[184:185], v25 offset:47616\n"
"buffer_load_dwordx4 v18, s[16:19], s51 offen offset:1024 lds\n"
"v_mfma_f32_4x4x4_16b_bf16 v[142:145], v[32:33], v[174:175], v[142:145]\n"
"v_mfma_f32_4x4x4_16b_bf16 v[150:153], v[32:33], v[186:187], v[150:153]\n"
"v_mfma_f32_4x4x4_16b_bf16 v[146:149], v[32:33], v[176:177], v[146:149]\n"
"v_mfma_f32_4x4x4_16b_bf16 v[154:157], v[32:33], v[188:189], v[154:157]\n"
"s_waitcnt lgkmcnt(7)\n"
"v_mfma_f32_32x32x16_bf16 v[78:93], a[248:251], v[166:169], v[78:93]\n"
"ds_read_b64 v[190:191], v36\n"
"ds_read_b64 v[192:193], v37\n"
"ds_read_b64 v[194:195], v38\n"
"s_waitcnt lgkmcnt(8)\n"
"v_mfma_f32_32x32x16_bf16 v[94:109], a[248:251], v[178:181], v[94:109]\n"
"ds_read_b64 v[196:197], v39\n"
"v_mfma_f32_4x4x4_16b_bf16 v[126:129], v[32:33], a[248:249], v[126:129]\n"
"v_mfma_f32_4x4x4_16b_bf16 v[134:137], a[248:249], a[248:249], v[134:137]\n"
"v_mfma_f32_4x4x4_16b_bf16 v[130:133], v[32:33], a[250:251], v[130:133]\n"
"v_mfma_f32_4x4x4_16b_bf16 v[138:141], a[250:251], a[250:251], v[138:141]\n"
"s_waitcnt lgkmcnt(6)\n"
"v_mfma_f32_32x32x16_bf16 v[78:93], a[252:255], v[170:173], v[78:93]\n"
"s_waitcnt lgkmcnt(4)\n"
"v_mfma_f32_32x32x16_bf16 v[94:109], a[252:255], v[182:185], v[94:109]\n"
"v_mfma_f32_4x4x4_16b_bf16 v[142:145], v[32:33], v[170:171], v[142:145]\n"
"v_mfma_f32_4x4x4_16b_bf16 v[150:153], v[32:33], v[182:183], v[150:153]\n"
"v_mfma_f32_4x4x4_16b_bf16 v[146:149], v[32:33], v[172:173], v[146:149]\n"
"v_mfma_f32_4x4x4_16b_bf16 v[154:157], v[32:33], v[184:185], v[154:157]\n"
"v_add_f32 v134, v134, v138\n"
"v_add_f32 v135, v135, v139\n"
"v_add_f32 v136, v136, v140\n"
"v_add_f32 v137, v137, v141\n"
"v_add_f32 v14, v126, v130\n"
"v_cndmask_b32 v134, v134, v135, s[28:29]\n"
"v_cndmask_b32 v136, v136, v137, s[28:29]\n"
"v_add_f32 v16, v142, v146\n"
"v_add_f32 v17, v150, v154\n"
"v_cndmask_b32 v15, v134, v136, s[30:31]\n"
"ds_write_b128 v40, v[14:17]\n"
"s_waitcnt vmcnt(8)\n"
"s_waitcnt lgkmcnt(0)\n"
"s_barrier\n"
"ds_read_b64 v[20:21], v41\n"
"ds_read_b32 v22, v43 offset:8\n"
"ds_read_b32 v23, v42 offset:12\n"
"ds_read_b64_tr_b16 v[166:167], v26 offset:0\n"
"ds_read_b64_tr_b16 v[168:169], v26 offset:512\n"
"ds_read_b64_tr_b16 v[170:171], v27 offset:4096\n"
"ds_read_b64_tr_b16 v[172:173], v27 offset:4608\n"
"ds_read_b64_tr_b16 v[174:175], v26 offset:8192\n"
"ds_read_b64_tr_b16 v[176:177], v26 offset:8704\n"
"s_waitcnt lgkmcnt(4)\n"
"v_mfma_f32_32x32x16_bf16 v[110:125], v[166:169], a[128:131], 0\n"
"ds_read_b64_tr_b16 v[178:179], v27 offset:12288\n"
"ds_read_b64_tr_b16 v[180:181], v27 offset:12800\n"
"s_waitcnt lgkmcnt(4)\n"
"v_mfma_f32_32x32x16_bf16 v[110:125], v[170:173], a[132:135], v[110:125]\n"
"ds_read_b64_tr_b16 v[182:183], v26 offset:2048\n"
"ds_read_b64_tr_b16 v[184:185], v26 offset:2560\n"
"v_mfma_f32_4x4x4_16b_bf16 v[158:161], v[32:33], v[170:171], 0\n"
"v_mfma_f32_4x4x4_16b_bf16 v[162:165], v[32:33], v[172:173], 0\n"
"s_waitcnt lgkmcnt(4)\n"
"v_mfma_f32_32x32x16_bf16 v[110:125], v[174:177], a[136:139], v[110:125]\n"
"ds_read_b64_tr_b16 v[186:187], v27 offset:6144\n"
"ds_read_b64_tr_b16 v[188:189], v27 offset:6656\n"
"v_add_f32 v14, v14, v20\n"
"v_add_f32 v15, v15, v21\n"
"v_add_f32 v16, v16, v22\n"
"v_add_f32 v17, v17, v23\n"
"v_mov_b32 v5, v14\n"
"v_mov_b32 v6, v15\n"
"s_nop 0\n"
"v_permlane32_swap_b32 v14, v5\n"
"v_permlane32_swap_b32 v15, v6\n"
"v_add_f32 v14, v14, v5\n"
"v_add_f32 v15, v15, v6\n"
"s_waitcnt lgkmcnt(4)\n"
"v_mfma_f32_32x32x16_bf16 v[110:125], v[178:181], a[140:143], v[110:125]\n"
"ds_read_b64_tr_b16 v[166:167], v26 offset:10240\n"
"ds_read_b64_tr_b16 v[168:169], v26 offset:10752\n"
"v_mul_f32 v13, 0xbb000000, v14\n"
"v_mul_f32 v5, v13, v13\n"
"v_fma_f32 v5, v15, v63, -v5\n"
"v_max_f32 v5, 0, v5\n"
"v_add_f32 v5, 925353388, v5\n"
"v_rsq_f32 v28, v5\n"
"v_mfma_f32_32x32x2_f32 v[78:93], v13, v16, v[78:93]\n"
"v_mfma_f32_4x4x4_16b_bf16 v[158:161], v[32:33], v[178:179], v[158:161]\n"
"v_mfma_f32_4x4x4_16b_bf16 v[162:165], v[32:33], v[180:181], v[162:165]\n"
"s_waitcnt lgkmcnt(4)\n"
"v_mfma_f32_32x32x16_bf16 v[110:125], v[182:185], a[144:147], v[110:125]\n"
"ds_read_b64_tr_b16 v[170:171], v27 offset:14336\n"
"ds_read_b64_tr_b16 v[172:173], v27 offset:14848\n"
"ds_write_b32 v44, v28\n"
"ds_read_b128 v[126:129], v45 offset:0\n"
"ds_read_b128 v[130:133], v45 offset:32\n"
"ds_read_b128 v[134:137], v45 offset:64\n"
"ds_read_b128 v[138:141], v45 offset:96\n"
"v_mfma_f32_32x32x2_f32 v[94:109], v13, v17, v[94:109]\n"
"s_waitcnt lgkmcnt(9)\n"
"v_mfma_f32_32x32x16_bf16 v[110:125], v[186:189], a[148:151], v[110:125]\n"
"ds_read_b64_tr_b16 v[174:175], v26 offset:16384\n"
"ds_read_b64_tr_b16 v[176:177], v26 offset:16896\n"
"v_mfma_f32_4x4x4_16b_bf16 v[158:161], v[32:33], v[186:187], v[158:161]\n"
"v_mfma_f32_4x4x4_16b_bf16 v[162:165], v[32:33], v[188:189], v[162:165]\n"
"s_waitcnt lgkmcnt(9)\n"
"v_mfma_f32_32x32x16_bf16 v[110:125], v[166:169], a[152:155], v[110:125]\n"
"ds_read_b64_tr_b16 v[178:179], v27 offset:20480\n"
"ds_read_b64_tr_b16 v[180:181], v27 offset:20992\n"
"s_waitcnt lgkmcnt(7)\n"
"v_fma_f32 v78, v78, v126, v62\n"
"v_fma_f32 v79, v79, v127, v62\n"
"v_fma_f32 v80, v80, v128, v62\n"
"v_fma_f32 v81, v81, v129, v62\n"
"v_mul_f32 v36, s34, v78\n"
"v_mfma_f32_32x32x16_bf16 v[110:125], v[170:173], a[156:159], v[110:125]\n"
"ds_read_b64_tr_b16 v[182:183], v26 offset:24576\n"
"ds_read_b64_tr_b16 v[184:185], v26 offset:25088\n"
"v_mul_f32 v37, s34, v79\n"
"v_mul_f32 v38, s34, v80\n"
"v_mul_f32 v39, s34, v81\n"
"v_exp_f32_e64 v36, v36 clamp\n"
"v_max_f32 v78, v62, v78\n"
"v_mfma_f32_4x4x4_16b_bf16 v[158:161], v[32:33], v[170:171], v[158:161]\n"
"v_mfma_f32_4x4x4_16b_bf16 v[162:165], v[32:33], v[172:173], v[162:165]\n"
"s_waitcnt lgkmcnt(4)\n"
"v_mfma_f32_32x32x16_bf16 v[110:125], v[174:177], a[160:163], v[110:125]\n"
"ds_read_b64_tr_b16 v[186:187], v27 offset:28672\n"
"ds_read_b64_tr_b16 v[188:189], v27 offset:29184\n"
"v_exp_f32_e64 v37, v37 clamp\n"
"v_max_f32 v79, v62, v79\n"
"v_exp_f32_e64 v38, v38 clamp\n"
"v_max_f32 v80, v62, v80\n"
"v_exp_f32_e64 v39, v39 clamp\n"
"s_waitcnt lgkmcnt(4)\n"
"v_mfma_f32_32x32x16_bf16 v[110:125], v[178:181], a[164:167], v[110:125]\n"
"ds_read_b64_tr_b16 v[166:167], v26 offset:18432\n"
"ds_read_b64_tr_b16 v[168:169], v26 offset:18944\n"
"v_max_f32 v81, v62, v81\n"
"v_add_f32 v78, v78, v36\n"
"v_add_f32 v79, v79, v37\n"
"v_add_f32 v80, v80, v38\n"
"v_add_f32 v81, v81, v39\n"
"v_mfma_f32_4x4x4_16b_bf16 v[158:161], v[32:33], v[178:179], v[158:161]\n"
"v_mfma_f32_4x4x4_16b_bf16 v[162:165], v[32:33], v[180:181], v[162:165]\n"
"s_waitcnt lgkmcnt(4)\n"
"v_mfma_f32_32x32x16_bf16 v[110:125], v[182:185], a[168:171], v[110:125]\n"
"ds_read_b64_tr_b16 v[170:171], v27 offset:22528\n"
"ds_read_b64_tr_b16 v[172:173], v27 offset:23040\n"
"v_mul_f32 v94, v94, v126\n"
"v_mul_f32 v95, v95, v127\n"
"v_mul_f32 v96, v96, v128\n"
"v_mul_f32 v97, v97, v129\n"
"v_cvt_pk_bf16_f32 v142, v78, v79\n"
"s_waitcnt lgkmcnt(4)\n"
"v_mfma_f32_32x32x16_bf16 v[110:125], v[186:189], a[172:175], v[110:125]\n"
"ds_read_b64_tr_b16 v[174:175], v26 offset:26624\n"
"ds_read_b64_tr_b16 v[176:177], v26 offset:27136\n"
"v_cvt_pk_bf16_f32 v143, v80, v81\n"
"v_cvt_pk_bf16_f32 v150, v94, v95\n"
"v_cvt_pk_bf16_f32 v151, v96, v97\n"
"v_fma_f32 v82, v82, v130, v62\n"
"v_fma_f32 v83, v83, v131, v62\n"
"v_mfma_f32_4x4x4_16b_bf16 v[158:161], v[32:33], v[186:187], v[158:161]\n"
"v_mfma_f32_4x4x4_16b_bf16 v[162:165], v[32:33], v[188:189], v[162:165]\n"
"s_waitcnt lgkmcnt(4)\n"
"v_mfma_f32_32x32x16_bf16 v[110:125], v[166:169], a[176:179], v[110:125]\n"
"ds_read_b64_tr_b16 v[178:179], v27 offset:30720\n"
"ds_read_b64_tr_b16 v[180:181], v27 offset:31232\n"
"v_fma_f32 v84, v84, v132, v62\n"
"v_fma_f32 v85, v85, v133, v62\n"
"v_mul_f32 v36, s34, v82\n"
"v_mul_f32 v37, s34, v83\n"
"v_mul_f32 v38, s34, v84\n"
"s_waitcnt vmcnt(0)\n"
"s_waitcnt lgkmcnt(0)\n"
"s_barrier\n"
"v_mfma_f32_32x32x16_bf16 v[110:125], v[170:173], a[180:183], v[110:125]\n"
"ds_read_b64_tr_b16 v[182:183], v24 offset:0\n"
"ds_read_b64_tr_b16 v[184:185], v24 offset:512\n"
"v_mul_f32 v39, s34, v85\n"
"v_exp_f32_e64 v36, v36 clamp\n"
"v_max_f32 v82, v62, v82\n"
"v_exp_f32_e64 v37, v37 clamp\n"
"v_max_f32 v83, v62, v83\n"
"v_mfma_f32_4x4x4_16b_bf16 v[158:161], v[32:33], v[170:171], v[158:161]\n"
"v_mfma_f32_4x4x4_16b_bf16 v[162:165], v[32:33], v[172:173], v[162:165]\n"
"v_mfma_f32_32x32x16_bf16 v[110:125], v[174:177], a[184:187], v[110:125]\n"
"ds_read_b64_tr_b16 v[186:187], v25 offset:4096\n"
"ds_read_b64_tr_b16 v[188:189], v25 offset:4608\n"
"v_exp_f32_e64 v38, v38 clamp\n"
"v_max_f32 v84, v62, v84\n"
"v_exp_f32_e64 v39, v39 clamp\n"
"v_max_f32 v85, v62, v85\n"
"v_add_f32 v82, v82, v36\n"
"v_mfma_f32_32x32x16_bf16 v[110:125], v[178:181], a[188:191], v[110:125]\n"
"ds_read_b64_tr_b16 v[166:167], v24 offset:8192\n"
"ds_read_b64_tr_b16 v[168:169], v24 offset:8704\n"
"v_add_f32 v83, v83, v37\n"
"v_add_f32 v84, v84, v38\n"
"v_add_f32 v85, v85, v39\n"
"v_mul_f32 v98, v98, v130\n"
"v_mul_f32 v99, v99, v131\n"
"v_mfma_f32_4x4x4_16b_bf16 v[158:161], v[32:33], v[178:179], v[158:161]\n"
"v_mfma_f32_4x4x4_16b_bf16 v[162:165], v[32:33], v[180:181], v[162:165]\n"
"s_waitcnt lgkmcnt(4)\n"
"v_mfma_f32_32x32x16_bf16 v[110:125], v[182:185], a[192:195], v[110:125]\n"
"ds_read_b64_tr_b16 v[170:171], v25 offset:12288\n"
"ds_read_b64_tr_b16 v[172:173], v25 offset:12800\n"
"v_mul_f32 v100, v100, v132\n"
"v_mul_f32 v101, v101, v133\n"
"v_cvt_pk_bf16_f32 v144, v82, v83\n"
"v_cvt_pk_bf16_f32 v145, v84, v85\n"
"v_cvt_pk_bf16_f32 v152, v98, v99\n"
"s_waitcnt lgkmcnt(4)\n"
"v_mfma_f32_32x32x16_bf16 v[110:125], v[186:189], a[196:199], v[110:125]\n"
"ds_read_b64_tr_b16 v[174:175], v24 offset:2048\n"
"ds_read_b64_tr_b16 v[176:177], v24 offset:2560\n"
"v_cvt_pk_bf16_f32 v153, v100, v101\n"
"s_nop 1\n"
"v_mfma_f32_32x32x16_bf16 v[198:213], v[142:145], v[150:153], 0\n"
"v_mfma_f32_32x32x16_bf16 v[214:229], v[142:145], v[32:35], 0\n"
"v_fma_f32 v86, v86, v134, v62\n"
"v_fma_f32 v87, v87, v135, v62\n"
"v_mfma_f32_4x4x4_16b_bf16 v[158:161], v[32:33], v[186:187], v[158:161]\n"
"v_mfma_f32_4x4x4_16b_bf16 v[162:165], v[32:33], v[188:189], v[162:165]\n"
"s_waitcnt lgkmcnt(4)\n"
"v_mfma_f32_32x32x16_bf16 v[110:125], v[166:169], a[200:203], v[110:125]\n"
"ds_read_b64_tr_b16 v[178:179], v25 offset:6144\n"
"ds_read_b64_tr_b16 v[180:181], v25 offset:6656\n"
"v_fma_f32 v88, v88, v136, v62\n"
"v_fma_f32 v89, v89, v137, v62\n"
"v_mul_f32 v36, s34, v86\n"
"v_mul_f32 v37, s34, v87\n"
"v_mul_f32 v38, s34, v88\n"
"s_waitcnt lgkmcnt(4)\n"
"v_mfma_f32_32x32x16_bf16 v[110:125], v[170:173], a[204:207], v[110:125]\n"
"ds_read_b64_tr_b16 v[182:183], v24 offset:10240\n"
"ds_read_b64_tr_b16 v[184:185], v24 offset:10752\n"
"v_mul_f32 v39, s34, v89\n"
"v_exp_f32_e64 v36, v36 clamp\n"
"v_max_f32 v86, v62, v86\n"
"v_exp_f32_e64 v37, v37 clamp\n"
"v_max_f32 v87, v62, v87\n"
"v_mfma_f32_4x4x4_16b_bf16 v[158:161], v[32:33], v[170:171], v[158:161]\n"
"v_mfma_f32_4x4x4_16b_bf16 v[162:165], v[32:33], v[172:173], v[162:165]\n"
"s_waitcnt lgkmcnt(4)\n"
"v_mfma_f32_32x32x16_bf16 v[110:125], v[174:177], a[208:211], v[110:125]\n"
"ds_read_b64_tr_b16 v[186:187], v25 offset:14336\n"
"ds_read_b64_tr_b16 v[188:189], v25 offset:14848\n"
"v_exp_f32_e64 v38, v38 clamp\n"
"v_max_f32 v88, v62, v88\n"
"v_exp_f32_e64 v39, v39 clamp\n"
"v_max_f32 v89, v62, v89\n"
"v_add_f32 v86, v86, v36\n"
"s_waitcnt lgkmcnt(4)\n"
"v_mfma_f32_32x32x16_bf16 v[110:125], v[178:181], a[212:215], v[110:125]\n"
"ds_read_b64_tr_b16 v[166:167], v24 offset:16384\n"
"ds_read_b64_tr_b16 v[168:169], v24 offset:16896\n"
"v_add_f32 v87, v87, v37\n"
"v_add_f32 v88, v88, v38\n"
"v_add_f32 v89, v89, v39\n"
"v_mul_f32 v102, v102, v134\n"
"v_mul_f32 v103, v103, v135\n"
"v_mfma_f32_4x4x4_16b_bf16 v[158:161], v[32:33], v[178:179], v[158:161]\n"
"v_mfma_f32_4x4x4_16b_bf16 v[162:165], v[32:33], v[180:181], v[162:165]\n"
"s_waitcnt lgkmcnt(4)\n"
"v_mfma_f32_32x32x16_bf16 v[110:125], v[182:185], a[216:219], v[110:125]\n"
"ds_read_b64_tr_b16 v[170:171], v25 offset:20480\n"
"ds_read_b64_tr_b16 v[172:173], v25 offset:20992\n"
"v_mul_f32 v104, v104, v136\n"
"v_mul_f32 v105, v105, v137\n"
"v_cvt_pk_bf16_f32 v146, v86, v87\n"
"v_cvt_pk_bf16_f32 v147, v88, v89\n"
"v_cvt_pk_bf16_f32 v154, v102, v103\n"
"s_waitcnt lgkmcnt(4)\n"
"v_mfma_f32_32x32x16_bf16 v[110:125], v[186:189], a[220:223], v[110:125]\n"
"ds_read_b64_tr_b16 v[174:175], v24 offset:24576\n"
"ds_read_b64_tr_b16 v[176:177], v24 offset:25088\n"
"v_cvt_pk_bf16_f32 v155, v104, v105\n"
"v_fma_f32 v90, v90, v138, v62\n"
"v_fma_f32 v91, v91, v139, v62\n"
"v_fma_f32 v92, v92, v140, v62\n"
"v_fma_f32 v93, v93, v141, v62\n"
"v_mfma_f32_4x4x4_16b_bf16 v[158:161], v[32:33], v[186:187], v[158:161]\n"
"v_mfma_f32_4x4x4_16b_bf16 v[162:165], v[32:33], v[188:189], v[162:165]\n"
"s_waitcnt lgkmcnt(4)\n"
"v_mfma_f32_32x32x16_bf16 v[110:125], v[166:169], a[224:227], v[110:125]\n"
"ds_read_b64_tr_b16 v[178:179], v25 offset:28672\n"
"ds_read_b64_tr_b16 v[180:181], v25 offset:29184\n"
"v_mul_f32 v36, s34, v90\n"
"v_mul_f32 v37, s34, v91\n"
"v_mul_f32 v38, s34, v92\n"
"v_mul_f32 v39, s34, v93\n"
"v_exp_f32_e64 v36, v36 clamp\n"
"s_waitcnt lgkmcnt(4)\n"
"v_mfma_f32_32x32x16_bf16 v[110:125], v[170:173], a[228:231], v[110:125]\n"
"ds_read_b64_tr_b16 v[182:183], v24 offset:18432\n"
"ds_read_b64_tr_b16 v[184:185], v24 offset:18944\n"
"v_max_f32 v90, v62, v90\n"
"v_exp_f32_e64 v37, v37 clamp\n"
"v_max_f32 v91, v62, v91\n"
"v_exp_f32_e64 v38, v38 clamp\n"
"v_max_f32 v92, v62, v92\n"
"v_mfma_f32_4x4x4_16b_bf16 v[158:161], v[32:33], v[170:171], v[158:161]\n"
"v_mfma_f32_4x4x4_16b_bf16 v[162:165], v[32:33], v[172:173], v[162:165]\n"
"s_waitcnt lgkmcnt(4)\n"
"v_mfma_f32_32x32x16_bf16 v[110:125], v[174:177], a[232:235], v[110:125]\n"
"ds_read_b64_tr_b16 v[186:187], v25 offset:22528\n"
"ds_read_b64_tr_b16 v[188:189], v25 offset:23040\n"
"v_exp_f32_e64 v39, v39 clamp\n"
"v_max_f32 v93, v62, v93\n"
"v_add_f32 v90, v90, v36\n"
"v_add_f32 v91, v91, v37\n"
"v_add_f32 v92, v92, v38\n"
"s_waitcnt lgkmcnt(4)\n"
"v_mfma_f32_32x32x16_bf16 v[110:125], v[178:181], a[236:239], v[110:125]\n"
"ds_read_b64_tr_b16 v[166:167], v24 offset:26624\n"
"ds_read_b64_tr_b16 v[168:169], v24 offset:27136\n"
"v_add_f32 v93, v93, v39\n"
"v_mul_f32 v106, v106, v138\n"
"v_mul_f32 v107, v107, v139\n"
"v_mul_f32 v108, v108, v140\n"
"v_mul_f32 v109, v109, v141\n"
"v_mfma_f32_4x4x4_16b_bf16 v[158:161], v[32:33], v[178:179], v[158:161]\n"
"v_mfma_f32_4x4x4_16b_bf16 v[162:165], v[32:33], v[180:181], v[162:165]\n"
"s_waitcnt lgkmcnt(4)\n"
"v_mfma_f32_32x32x16_bf16 v[110:125], v[182:185], a[240:243], v[110:125]\n"
"ds_read_b64_tr_b16 v[170:171], v25 offset:30720\n"
"ds_read_b64_tr_b16 v[172:173], v25 offset:31232\n"
"v_cvt_pk_bf16_f32 v148, v90, v91\n"
"v_cvt_pk_bf16_f32 v149, v92, v93\n"
"v_cvt_pk_bf16_f32 v156, v106, v107\n"
"v_cvt_pk_bf16_f32 v157, v108, v109\n"
"s_waitcnt lgkmcnt(4)\n"
"v_mfma_f32_32x32x16_bf16 v[110:125], v[186:189], a[244:247], v[110:125]\n"
"v_mfma_f32_4x4x4_16b_bf16 v[158:161], v[32:33], v[186:187], v[158:161]\n"
"v_mfma_f32_4x4x4_16b_bf16 v[162:165], v[32:33], v[188:189], v[162:165]\n"
"v_mfma_f32_32x32x16_bf16 v[198:213], v[146:149], v[154:157], v[198:213]\n"
"v_mfma_f32_32x32x16_bf16 v[214:229], v[146:149], v[32:35], v[214:229]\n"
"s_waitcnt lgkmcnt(2)\n"
"v_mfma_f32_32x32x16_bf16 v[110:125], v[166:169], a[248:251], v[110:125]\n"
"s_waitcnt lgkmcnt(0)\n"
"v_mfma_f32_32x32x16_bf16 v[110:125], v[170:173], a[252:255], v[110:125]\n"
"v_mfma_f32_4x4x4_16b_bf16 v[158:161], v[32:33], v[170:171], v[158:161]\n"
"v_mfma_f32_4x4x4_16b_bf16 v[162:165], v[32:33], v[172:173], v[162:165]\n"
"s_nop 4\n"
"v_cndmask_b32 v202, v202, v210, s[32:33]\n"
"v_cndmask_b32 v203, v203, v211, s[32:33]\n"
"v_cndmask_b32 v204, v204, v212, s[32:33]\n"
"v_cndmask_b32 v205, v205, v213, s[32:33]\n"
"ds_write_b128 v46, v[202:205] offset:0\n"
"v_cndmask_b32 v218, v218, v226, s[32:33]\n"
"v_cndmask_b32 v219, v219, v227, s[32:33]\n"
"v_cndmask_b32 v220, v220, v228, s[32:33]\n"
"v_cndmask_b32 v221, v221, v229, s[32:33]\n"
"ds_write_b128 v46, v[218:221] offset:1024\n"
"v_add_f32 v158, v158, v162\n"
"ds_write_b32 v48, v158\n"
"v_cndmask_b32 v198, v198, v206, s[32:33]\n"
"v_cndmask_b32 v199, v199, v207, s[32:33]\n"
"v_cndmask_b32 v200, v200, v208, s[32:33]\n"
"v_cndmask_b32 v201, v201, v209, s[32:33]\n"
"v_cndmask_b32 v214, v214, v222, s[32:33]\n"
"v_cndmask_b32 v215, v215, v223, s[32:33]\n"
"v_cndmask_b32 v216, v216, v224, s[32:33]\n"
"v_cndmask_b32 v217, v217, v225, s[32:33]\n"
"s_waitcnt lgkmcnt(0)\n"
"s_barrier\n"
"ds_read_b32 v7, v49\n"
"ds_read_b128 v[202:205], v47\n"
"ds_read_b128 v[218:221], v47 offset:1024\n"
"s_waitcnt lgkmcnt(2)\n"
"v_add_f32 v158, v158, v7\n"
"s_nop 1\n"
"v_mfma_f32_32x32x2_f32 v[110:125], v158, v13, v[110:125]\n"
"s_waitcnt lgkmcnt(1)\n"
"v_pk_add_f32 v[64:65], v[198:199], v[202:203]\n"
"v_pk_add_f32 v[68:69], v[200:201], v[204:205]\n"
"buffer_store_dwordx4 v[64:67], v50, s[8:11], s65 offen sc1\n"
"buffer_store_dwordx4 v[68:71], v50, s[8:11], s65 offen offset:128 sc1\n"
"s_waitcnt lgkmcnt(0)\n"
"v_pk_add_f32 v[214:215], v[214:215], v[218:219]\n"
"v_pk_add_f32 v[216:217], v[216:217], v[220:221]\n"
"v_cndmask_b32 v72, v214, v216, s[28:29]\n"
"v_cndmask_b32 v73, v215, v217, s[28:29]\n"
"buffer_store_dwordx4 v[72:75], v51, s[8:11], s66 offen sc1\n"
"s_nop 6\n"
"v_pk_fma_f32 v[110:111], v[110:111], v[28:29], v[62:63] op_sel_hi:[1,0,0]\n"
"v_pk_fma_f32 v[112:113], v[112:113], v[28:29], v[62:63] op_sel_hi:[1,0,0]\n"
"v_pk_mul_f32 v[36:37], v[110:111], s[34:35]\n"
"v_pk_mul_f32 v[38:39], v[112:113], s[34:35]\n"
"v_exp_f32_e64 v36, v36 clamp\n"
"v_max_f32 v110, v62, v110\n"
"v_exp_f32_e64 v37, v37 clamp\n"
"v_max_f32 v111, v62, v111\n"
"v_exp_f32_e64 v38, v38 clamp\n"
"v_max_f32 v112, v62, v112\n"
"v_exp_f32_e64 v39, v39 clamp\n"
"v_max_f32 v113, v62, v113\n"
"v_pk_add_f32 v[110:111], v[110:111], v[36:37]\n"
"v_pk_add_f32 v[112:113], v[112:113], v[38:39]\n"
"v_cvt_pk_bf16_f32 v142, v110, v111\n"
"v_cvt_pk_bf16_f32 v143, v112, v113\n"
"s_add_u32 m0, s25, 0\n"
"s_nop 0\n"
"buffer_load_dwordx4 v58, s[20:23], s73 offen lds\n"
"v_pk_fma_f32 v[114:115], v[114:115], v[28:29], v[62:63] op_sel_hi:[1,0,0]\n"
"v_pk_fma_f32 v[116:117], v[116:117], v[28:29], v[62:63] op_sel_hi:[1,0,0]\n"
"v_pk_mul_f32 v[36:37], v[114:115], s[34:35]\n"
"v_pk_mul_f32 v[38:39], v[116:117], s[34:35]\n"
"v_exp_f32_e64 v36, v36 clamp\n"
"v_max_f32 v114, v62, v114\n"
"v_exp_f32_e64 v37, v37 clamp\n"
"v_max_f32 v115, v62, v115\n"
"v_exp_f32_e64 v38, v38 clamp\n"
"v_max_f32 v116, v62, v116\n"
"v_exp_f32_e64 v39, v39 clamp\n"
"v_max_f32 v117, v62, v117\n"
"v_pk_add_f32 v[114:115], v[114:115], v[36:37]\n"
"v_pk_add_f32 v[116:117], v[116:117], v[38:39]\n"
"v_cvt_pk_bf16_f32 v144, v114, v115\n"
"v_cvt_pk_bf16_f32 v145, v116, v117\n"
"buffer_store_dwordx4 v[142:145], v4, s[8:11], s69 offen offset:0 sc1\n"
"buffer_load_dwordx4 v59, s[20:23], s73 offen offset:1024 lds\n"
"v_pk_fma_f32 v[118:119], v[118:119], v[28:29], v[62:63] op_sel_hi:[1,0,0]\n"
"v_pk_fma_f32 v[120:121], v[120:121], v[28:29], v[62:63] op_sel_hi:[1,0,0]\n"
"v_pk_mul_f32 v[36:37], v[118:119], s[34:35]\n"
"v_pk_mul_f32 v[38:39], v[120:121], s[34:35]\n"
"v_exp_f32_e64 v36, v36 clamp\n"
"v_max_f32 v118, v62, v118\n"
"v_exp_f32_e64 v37, v37 clamp\n"
"v_max_f32 v119, v62, v119\n"
"v_exp_f32_e64 v38, v38 clamp\n"
"v_max_f32 v120, v62, v120\n"
"v_exp_f32_e64 v39, v39 clamp\n"
"v_max_f32 v121, v62, v121\n"
"v_pk_add_f32 v[118:119], v[118:119], v[36:37]\n"
"v_pk_add_f32 v[120:121], v[120:121], v[38:39]\n"
"v_cvt_pk_bf16_f32 v146, v118, v119\n"
"v_cvt_pk_bf16_f32 v147, v120, v121\n"
"buffer_load_dwordx4 v60, s[20:23], s73 offen offset:2048 lds\n"
"v_pk_fma_f32 v[122:123], v[122:123], v[28:29], v[62:63] op_sel_hi:[1,0,0]\n"
"v_pk_fma_f32 v[124:125], v[124:125], v[28:29], v[62:63] op_sel_hi:[1,0,0]\n"
"v_pk_mul_f32 v[36:37], v[122:123], s[34:35]\n"
"v_pk_mul_f32 v[38:39], v[124:125], s[34:35]\n"
"v_exp_f32_e64 v36, v36 clamp\n"
"v_max_f32 v122, v62, v122\n"
"v_exp_f32_e64 v37, v37 clamp\n"
"v_max_f32 v123, v62, v123\n"
"v_exp_f32_e64 v38, v38 clamp\n"
"v_max_f32 v124, v62, v124\n"
"v_exp_f32_e64 v39, v39 clamp\n"
"v_max_f32 v125, v62, v125\n"
"v_pk_add_f32 v[122:123], v[122:123], v[36:37]\n"
"v_pk_add_f32 v[124:125], v[124:125], v[38:39]\n"
"v_cvt_pk_bf16_f32 v148, v122, v123\n"
"v_cvt_pk_bf16_f32 v149, v124, v125\n"
"buffer_store_dwordx4 v[146:149], v4, s[8:11], s69 offen offset:1024 sc1\n"
"buffer_load_dwordx4 v[230:233], v4, s[8:11], s67 offen sc1\n"
"buffer_load_dwordx4 v[234:237], v4, s[8:11], s67 offen offset:1024 sc1\n"
"buffer_load_dwordx4 v[238:241], v52, s[8:11], s68 offen sc1\n"
"buffer_load_dwordx4 v61, s[20:23], s73 offen offset:3072 lds\n"
"s_add_u32 s73, s73, 0x10000\n"
"s_and_b32 s73, s73, 0x7ffff\n"
"s_add_u32 m0, s25, 16384\n"
"s_nop 0\n"
"buffer_load_dwordx4 v58, s[20:23], s73 offen lds\n"
"buffer_load_dwordx4 v59, s[20:23], s73 offen offset:1024 lds\n"
"buffer_load_dwordx4 v60, s[20:23], s73 offen offset:2048 lds\n"
"buffer_load_dwordx4 v61, s[20:23], s73 offen offset:3072 lds\n"
"s_add_u32 s73, s73, 0x10000\n"
"s_and_b32 s73, s73, 0x7ffff\n"
"s_waitcnt vmcnt(8)\n"
"buffer_store_dwordx2 v[76:77], v53, s[8:11], s70 offen sc1\n"
"s_waitcnt vmcnt(8)\n"
"v_cmp_ne_u64 vcc, s[12:13], v[232:233]\n"
"s_waitcnt vmcnt(7)\n"
"v_cmp_ne_u64 s[30:31], s[12:13], v[236:237]\n"
"s_waitcnt vmcnt(6)\n"
"v_cmp_ne_u64 s[32:33], s[12:13], v[240:241]\n"
"s_nop 1\n"
"s_or_b64 vcc, vcc, s[30:31]\n"
"s_nop 0\n"
"s_and_b64 s[32:33], s[32:33], s[74:75]\n"
"s_or_b64 vcc, vcc, s[32:33]\n"
"s_cbranch_vccnz .Lretry1_%=\n"
".Lback1_%=:\n"
"v_pk_add_f32 v[230:231], v[230:231], v[234:235]\n"
"v_mov_b32 v5, v230\n"
"v_mov_b32 v6, v231\n"
"v_add_f32_dpp v238, v238, v238 quad_perm:[2,3,0,1] row_mask:0xf bank_mask:0xf\n"
"v_add_f32_dpp v239, v239, v239 quad_perm:[2,3,0,1] row_mask:0xf bank_mask:0xf\n"
"v_permlane16_swap_b32 v230, v5\n"
"v_permlane16_swap_b32 v231, v6\n"
"v_add_f32 v230, v230, v5\n"
"v_add_f32 v231, v231, v6\n"
"v_mov_b32 v5, v230\n"
"v_mov_b32 v6, v231\n"
"v_add_f32_dpp v238, v238, v238 row_ror:4 row_mask:0xf bank_mask:0xf\n"
"v_add_f32_dpp v239, v239, v239 row_ror:4 row_mask:0xf bank_mask:0xf\n"
"v_permlane32_swap_b32 v230, v5\n"
"v_permlane32_swap_b32 v231, v6\n"
"v_add_f32 v230, v230, v5\n"
"v_add_f32 v231, v231, v6\n"
"ds_write_b64 v57, v[230:231]\n"
"v_add_f32_dpp v238, v238, v238 row_ror:8 row_mask:0xf bank_mask:0xf\n"
"v_add_f32_dpp v239, v239, v239 row_ror:8 row_mask:0xf bank_mask:0xf\n"
"s_waitcnt lgkmcnt(0)\n"
"s_barrier\n"
"ds_read_b64 v[198:199], v56 offset:0\n"
"ds_read_b64 v[200:201], v56 offset:128\n"
"ds_read_b64 v[202:203], v56 offset:256\n"
"ds_read_b64 v[204:205], v56 offset:384\n"
"v_mov_b32 v5, v238\n"
"v_mov_b32 v6, v239\n"
"s_nop 0\n"
"v_permlane16_swap_b32 v238, v5\n"
"v_permlane16_swap_b32 v239, v6\n"
"v_add_f32 v238, v238, v5\n"
"v_add_f32 v239, v239, v6\n"
"v_mov_b32 v5, v238\n"
"v_mov_b32 v6, v239\n"
"s_nop 0\n"
"v_permlane32_swap_b32 v238, v5\n"
"v_permlane32_swap_b32 v239, v6\n"
"v_add_f32 v238, v238, v5\n"
"v_add_f32 v239, v239, v6\n"
"s_waitcnt lgkmcnt(2)\n"
"v_pk_add_f32 v[198:199], v[198:199], v[200:201]\n"
"s_waitcnt lgkmcnt(0)\n"
"v_pk_add_f32 v[202:203], v[202:203], v[204:205]\n"
"v_cvt_pk_bf16_f32 v72, v238, v239\n"
"v_pk_add_f32 v[198:199], v[198:199], v[202:203]\n"
"v_cvt_pk_bf16_f32 v64, v198, v199\n"
"v_mov_b32_dpp v73, v72 quad_perm:[1,0,3,2] row_mask:0xf bank_mask:0xf\n"
"s_nop 0\n"
"v_mov_b32_dpp v65, v64 row_ror:8 row_mask:0xf bank_mask:0xf\n"
"buffer_store_dwordx4 v[64:67], v54, s[8:11], s71 offen sc1\n"
"buffer_store_dwordx4 v[72:75], v55, s[8:11], s72 offen sc1\n"
"s_add_u32 m0, s25, 32768\n"
"s_nop 0\n"
"buffer_load_dwordx4 v58, s[20:23], s73 offen lds\n"
"s_lshl_b32 s40, s15, 13\n"
"s_add_u32 s4, s40, 528384\n"
"s_add_u32 s5, s40, 532480\n"
"s_add_u32 s6, s25, 561152\n"
"buffer_load_dwordx4 v59, s[20:23], s73 offen offset:1024 lds\n"
"s_lshl_b32 s41, s62, 15\n"
"s_add_u32 s41, s41, s40\n"
"s_add_u32 s16, s41, 4194304\n"
"s_add_u32 s17, s41, 4198400\n"
"buffer_load_dwordx4 v60, s[20:23], s73 offen offset:2048 lds\n"
"s_lshl2_add_u32 s40, s38, s15\n"
"s_and_b32 s41, s62, 1\n"
"s_lshl3_add_u32 s40, s40, s41\n"
"s_lshl_b32 s40, s40, 7\n"
"buffer_load_dwordx4 v61, s[20:23], s73 offen offset:3072 lds\n"
"s_add_u32 s73, s73, 0x10000\n"
"s_and_b32 s73, s73, 0x7ffff\n"
"s_add_u32 s7, s40, 4096\n"
"v_and_b32 v20, 3, v1\n"
"v_lshlrev_b32 v20, 8, v20\n"
"v_lshlrev_b32 v16, 7, v2\n"
"s_add_u32 m0, s25, 49152\n"
"s_nop 0\n"
"buffer_load_dwordx4 v58, s[20:23], s73 offen lds\n"
"v_lshlrev_b32 v24, 3, v2\n"
"s_lshl_b32 s40, s15, 8\n"
"s_add_u32 s40, s40, 162304\n"
"v_add_u32 v17, s40, v24\n"
"buffer_load_dwordx4 v59, s[20:23], s73 offen offset:1024 lds\n"
"v_lshrrev_b32 v5, 4, v1\n"
"v_lshl_add_u32 v18, v5, 3, s40\n"
"v_bfe_u32 v5, v1, 2, 2\n"
"v_lshl_add_u32 v6, v3, 2, v5\n"
"buffer_load_dwordx4 v60, s[20:23], s73 offen offset:2048 lds\n"
"v_lshl_add_u32 v6, v6, 8, v19\n"
"v_xor_b32 v5, s15, v5\n"
"v_lshl_add_u32 v13, v5, 6, v6\n"
"v_add_u32 v14, 0x10000, v13\n"
"buffer_load_dwordx4 v61, s[20:23], s73 offen offset:3072 lds\n"
"s_add_u32 s73, s73, 0x10000\n"
"s_and_b32 s73, s73, 0x7ffff\n"
"v_add_u32 v15, 0x20000, v13\n"
"s_lshl_b32 s40, s15, 1\n"
"s_sub_u32 s40, s40, s64\n"
"s_and_b32 s41, s40, 7\n"
"s_add_u32 m0, s25, 98304\n"
"s_nop 0\n"
"buffer_load_dwordx4 v58, s[20:23], s73 offen lds\n"
"s_lshl_b32 s41, s41, 12\n"
"s_add_u32 s41, s41, 126976\n"
"v_add_u32 v21, s41, v4\n"
"s_add_u32 s40, s40, 1\n"
"buffer_load_dwordx4 v59, s[20:23], s73 offen offset:1024 lds\n"
"s_and_b32 s41, s40, 7\n"
"s_lshl_b32 s41, s41, 12\n"
"s_add_u32 s41, s41, 126976\n"
"v_add_u32 v22, s41, v4\n"
"buffer_load_dwordx4 v60, s[20:23], s73 offen offset:2048 lds\n"
"v_add_u32 v23, 126976, v4\n"
"s_lshl2_add_u32 s40, s62, s63\n"
"s_lshl2_add_u32 s40, s40, s15\n"
"s_lshl_b32 s41, s40, 11\n"
"buffer_load_dwordx4 v61, s[20:23], s73 offen offset:3072 lds\n"
"s_add_u32 s73, s73, 0x10000\n"
"s_and_b32 s73, s73, 0x7ffff\n"
"s_add_u32 s18, s41, 6291456\n"
"s_lshl_b32 s41, s40, 8\n"
"s_add_u32 s19, s41, 8388608\n"
"s_lshl_b32 s41, s40, 7\n"
"s_add_u32 m0, s25, 114688\n"
"s_nop 0\n"
"buffer_load_dwordx4 v58, s[20:23], s73 offen lds\n"
"s_add_u32 s24, s41, 135168\n"
"v_lshlrev_b32 v26, 16, v190\n"
"v_and_b32 v27, 0xffff0000, v190\n"
"v_lshlrev_b32 v28, 16, v191\n"
"buffer_load_dwordx4 v59, s[20:23], s73 offen offset:1024 lds\n"
"v_and_b32 v29, 0xffff0000, v191\n"
"v_lshlrev_b32 v30, 16, v192\n"
"v_and_b32 v31, 0xffff0000, v192\n"
"v_lshlrev_b32 v32, 16, v193\n"
"buffer_load_dwordx4 v60, s[20:23], s73 offen offset:2048 lds\n"
"v_and_b32 v33, 0xffff0000, v193\n"
"v_lshlrev_b32 v34, 16, v194\n"
"v_and_b32 v35, 0xffff0000, v194\n"
"v_lshlrev_b32 v36, 16, v195\n"
"buffer_load_dwordx4 v61, s[20:23], s73 offen offset:3072 lds\n"
"s_add_u32 s73, s73, 0x10000\n"
"s_and_b32 s73, s73, 0x7ffff\n"
"v_and_b32 v37, 0xffff0000, v195\n"
"v_lshlrev_b32 v38, 16, v196\n"
"v_and_b32 v39, 0xffff0000, v196\n"
"v_lshlrev_b32 v40, 16, v197\n"
"s_add_u32 m0, s25, 131072\n"
"s_nop 0\n"
"buffer_load_dwordx4 v58, s[20:23], s73 offen lds\n"
"v_and_b32 v41, 0xffff0000, v197\n"
"buffer_load_dwordx4 v59, s[20:23], s73 offen offset:1024 lds\n"
"buffer_load_dwordx4 v60, s[20:23], s73 offen offset:2048 lds\n"
"buffer_load_dwordx4 v61, s[20:23], s73 offen offset:3072 lds\n"
"s_add_u32 s73, s73, 0x10000\n"
"s_and_b32 s73, s73, 0x7ffff\n"
"s_add_u32 m0, s25, 65536\n"
"s_nop 0\n"
"buffer_load_dwordx4 v58, s[20:23], s73 offen lds\n"
"buffer_load_dwordx4 v59, s[20:23], s73 offen offset:1024 lds\n"
"buffer_load_dwordx4 v60, s[20:23], s73 offen offset:2048 lds\n"
"buffer_load_dwordx4 v61, s[20:23], s73 offen offset:3072 lds\n"
"s_add_u32 s73, s73, 0x10000\n"
"s_and_b32 s73, s73, 0x7ffff\n"
"s_lshl3_add_u32 s40, s60, 24\n"
"s_load_dwordx2 s[52:53], %0, s40\n"
"s_mov_b32 s54, 1835008\n"
"s_mov_b32 s55, 0x20000\n"
"s_waitcnt vmcnt(0)\n"
"s_waitcnt lgkmcnt(0)\n"
"s_barrier\n"
"buffer_load_dwordx2 v[50:51], v20, s[8:11], s7 offen sc1\n"
"buffer_load_dwordx4 v[86:89], v16, s[8:11], s6 offen sc1\n"
"ds_read_b64_tr_b16 a[0:1], v13 offset:0\n"
"ds_read_b64_tr_b16 a[2:3], v13 offset:2048\n"
"ds_read_b64_tr_b16 a[4:5], v13 offset:4096\n"
"ds_read_b64_tr_b16 a[6:7], v13 offset:6144\n"
"ds_read_b64_tr_b16 a[8:9], v13 offset:8192\n"
"ds_read_b64_tr_b16 a[10:11], v13 offset:10240\n"
"ds_read_b64_tr_b16 a[12:13], v13 offset:12288\n"
"buffer_load_dwordx4 v[42:45], v4, s[8:11], s4 offen offset:0 sc1\n"
"ds_read_b64_tr_b16 a[14:15], v13 offset:14336\n"
"ds_read_b64_tr_b16 a[16:17], v13 offset:16384\n"
"ds_read_b64_tr_b16 a[18:19], v13 offset:18432\n"
"ds_read_b64_tr_b16 a[20:21], v13 offset:20480\n"
"ds_read_b64_tr_b16 a[22:23], v13 offset:22528\n"
"ds_read_b64_tr_b16 a[24:25], v13 offset:24576\n"
"ds_read_b64_tr_b16 a[26:27], v13 offset:26624\n"
"buffer_load_dwordx4 v[46:49], v4, s[8:11], s4 offen offset:1024 sc1\n"
"ds_read_b64_tr_b16 a[28:29], v13 offset:28672\n"
"ds_read_b64_tr_b16 a[30:31], v13 offset:30720\n"
"ds_read_b64_tr_b16 a[32:33], v13 offset:32768\n"
"ds_read_b64_tr_b16 a[34:35], v13 offset:34816\n"
"ds_read_b64_tr_b16 a[36:37], v13 offset:36864\n"
"ds_read_b64_tr_b16 a[38:39], v13 offset:38912\n"
"ds_read_b64_tr_b16 a[40:41], v13 offset:40960\n"
"buffer_load_dwordx4 v[54:57], v4, s[8:11], s4 offen offset:2048 sc1\n"
"ds_read_b64_tr_b16 a[42:43], v13 offset:43008\n"
"ds_read_b64_tr_b16 a[44:45], v13 offset:45056\n"
"ds_read_b64_tr_b16 a[46:47], v13 offset:47104\n"
"ds_read_b64_tr_b16 a[48:49], v13 offset:49152\n"
"ds_read_b64_tr_b16 a[50:51], v13 offset:51200\n"
"ds_read_b64_tr_b16 a[52:53], v13 offset:53248\n"
"ds_read_b64_tr_b16 a[54:55], v13 offset:55296\n"
"buffer_load_dwordx4 v[62:65], v4, s[8:11], s4 offen offset:3072 sc1\n"
"ds_read_b64_tr_b16 a[56:57], v13 offset:57344\n"
"ds_read_b64_tr_b16 a[58:59], v13 offset:59392\n"
"ds_read_b64_tr_b16 a[60:61], v13 offset:61440\n"
"ds_read_b64_tr_b16 a[62:63], v13 offset:63488\n"
"ds_read_b64_tr_b16 a[64:65], v14 offset:32768\n"
"ds_read_b64_tr_b16 a[66:67], v14 offset:34816\n"
"ds_read_b64_tr_b16 a[68:69], v14 offset:36864\n"
"buffer_load_dwordx4 v[66:69], v4, s[8:11], s5 offen offset:0 sc1\n"
"ds_read_b64_tr_b16 a[70:71], v14 offset:38912\n"
"ds_read_b64_tr_b16 a[72:73], v14 offset:40960\n"
"ds_read_b64_tr_b16 a[74:75], v14 offset:43008\n"
"ds_read_b64_tr_b16 a[76:77], v14 offset:45056\n"
"ds_read_b64_tr_b16 a[78:79], v14 offset:47104\n"
"ds_read_b64_tr_b16 a[80:81], v14 offset:49152\n"
"ds_read_b64_tr_b16 a[82:83], v14 offset:51200\n"
"buffer_load_dwordx4 v[70:73], v4, s[8:11], s5 offen offset:1024 sc1\n"
"ds_read_b64_tr_b16 a[84:85], v14 offset:53248\n"
"ds_read_b64_tr_b16 a[86:87], v14 offset:55296\n"
"ds_read_b64_tr_b16 a[88:89], v14 offset:57344\n"
"ds_read_b64_tr_b16 a[90:91], v14 offset:59392\n"
"ds_read_b64_tr_b16 a[92:93], v14 offset:61440\n"
"ds_read_b64_tr_b16 a[94:95], v14 offset:63488\n"
"ds_read_b64_tr_b16 a[96:97], v15 offset:0\n"
"buffer_load_dwordx4 v[78:81], v4, s[8:11], s5 offen offset:2048 sc1\n"
"ds_read_b64_tr_b16 a[98:99], v15 offset:2048\n"
"ds_read_b64_tr_b16 a[100:101], v15 offset:4096\n"
"ds_read_b64_tr_b16 a[102:103], v15 offset:6144\n"
"ds_read_b64_tr_b16 a[104:105], v15 offset:8192\n"
"ds_read_b64_tr_b16 a[106:107], v15 offset:10240\n"
"ds_read_b64_tr_b16 a[108:109], v15 offset:12288\n"
"ds_read_b64_tr_b16 a[110:111], v15 offset:14336\n"
"buffer_load_dwordx4 v[82:85], v4, s[8:11], s5 offen offset:3072 sc1\n"
"ds_read_b64_tr_b16 a[112:113], v14 offset:0\n"
"ds_read_b64_tr_b16 a[114:115], v14 offset:2048\n"
"ds_read_b64_tr_b16 a[116:117], v14 offset:4096\n"
"ds_read_b64_tr_b16 a[118:119], v14 offset:6144\n"
"ds_read_b64_tr_b16 a[120:121], v14 offset:8192\n"
"ds_read_b64_tr_b16 a[122:123], v14 offset:10240\n"
"ds_read_b64_tr_b16 a[124:125], v14 offset:12288\n"
"ds_read_b64_tr_b16 a[126:127], v14 offset:14336\n"
"s_waitcnt lgkmcnt(0)\n"
"s_barrier\n"
"s_waitcnt vmcnt(9)\n"
"v_cmp_ne_u64 vcc, s[12:13], v[50:51]\n"
"s_nop 4\n"
"s_cbranch_vccnz .Lretry2_%=\n"
".Lback2_%=:\n"
"buffer_load_dwordx4 a[128:131], v4, s[8:11], s16 offen offset:0 sc1\n"
"buffer_load_dwordx4 a[132:135], v4, s[8:11], s16 offen offset:1024 sc1\n"
"buffer_load_dwordx4 a[136:139], v4, s[8:11], s16 offen offset:2048 sc1\n"
"buffer_load_dwordx4 a[140:143], v4, s[8:11], s16 offen offset:3072 sc1\n"
"buffer_load_dwordx4 a[144:147], v4, s[8:11], s17 offen offset:0 sc1\n"
"buffer_load_dwordx4 a[148:151], v4, s[8:11], s17 offen offset:1024 sc1\n"
"buffer_load_dwordx4 a[152:155], v4, s[8:11], s17 offen offset:2048 sc1\n"
"buffer_load_dwordx4 a[156:159], v4, s[8:11], s17 offen offset:3072 sc1\n"
"s_waitcnt vmcnt(16)\n"
"v_cmp_ne_u64 vcc, s[12:13], v[88:89]\n"
"s_waitcnt vmcnt(15)\n"
"v_cmp_ne_u64 s[30:31], s[12:13], v[44:45]\n"
"s_waitcnt vmcnt(14)\n"
"v_cmp_ne_u64 s[32:33], s[12:13], v[48:49]\n"
"s_waitcnt vmcnt(13)\n"
"v_cmp_ne_u64 s[34:35], s[12:13], v[56:57]\n"
"s_or_b64 vcc, vcc, s[30:31]\n"
"s_nop 0\n"
"s_or_b64 vcc, vcc, s[32:33]\n"
"s_nop 0\n"
"s_or_b64 vcc, vcc, s[34:35]\n"
"s_waitcnt vmcnt(12)\n"
"v_cmp_ne_u64 s[28:29], s[12:13], v[64:65]\n"
"s_waitcnt vmcnt(11)\n"
"v_cmp_ne_u64 s[30:31], s[12:13], v[68:69]\n"
"s_waitcnt vmcnt(10)\n"
"v_cmp_ne_u64 s[32:33], s[12:13], v[72:73]\n"
"s_waitcnt vmcnt(9)\n"
"v_cmp_ne_u64 s[34:35], s[12:13], v[80:81]\n"
"s_or_b64 vcc, vcc, s[28:29]\n"
"s_or_b64 vcc, vcc, s[30:31]\n"
"s_or_b64 vcc, vcc, s[32:33]\n"
"s_nop 0\n"
"s_or_b64 vcc, vcc, s[34:35]\n"
"s_waitcnt vmcnt(8)\n"
"v_cmp_ne_u64 s[28:29], s[12:13], v[84:85]\n"
"s_nop 3\n"
"s_or_b64 vcc, vcc, s[28:29]\n"
"s_cbranch_vccnz .Lretry3_%=\n"
".Lback3_%=:\n"
"s_cmp_eq_u32 %1, 0\n"
"s_cselect_b32 s40, 0, 2147483648\n"
"s_or_b32 s40, s40, s15\n"
"s_cmp_eq_u32 s40, 0\n"
"s_cselect_b32 s40, 0, 2147483648\n"
"v_or_b32 v5, s40, v53\n"
"ds_write_b64 v17, v[86:87]\n"
"ds_read_b64 v[44:45], v18 offset:0\n"
"ds_read_b64 v[48:49], v18 offset:32\n"
"ds_read_b64 v[56:57], v18 offset:64\n"
"ds_read_b64 v[64:65], v18 offset:96\n"
"ds_read_b64 v[68:69], v18 offset:128\n"
"ds_read_b64 v[72:73], v18 offset:160\n"
"ds_read_b64 v[80:81], v18 offset:192\n"
"ds_read_b64 v[84:85], v18 offset:224\n"
"s_waitcnt lgkmcnt(7)\n"
"v_permlane16_swap_b32 v42, v44\n"
"v_permlane16_swap_b32 v43, v45\n"
"s_nop 0\n"
"s_waitcnt vmcnt(7)\n"
"v_mfma_f32_32x32x16_bf16 v[90:105], v[42:45], a[128:131], 0\n"
"s_waitcnt lgkmcnt(6)\n"
"v_permlane16_swap_b32 v46, v48\n"
"v_permlane16_swap_b32 v47, v49\n"
"s_nop 0\n"
"s_waitcnt vmcnt(6)\n"
"v_mfma_f32_32x32x16_bf16 v[106:121], v[46:49], a[132:135], 0\n"
"s_waitcnt lgkmcnt(5)\n"
"v_permlane16_swap_b32 v54, v56\n"
"v_permlane16_swap_b32 v55, v57\n"
"s_nop 0\n"
"s_waitcnt vmcnt(5)\n"
"v_mfma_f32_32x32x16_bf16 v[122:137], v[54:57], a[136:139], 0\n"
"v_add_f32 v98, 897988541, v98\n"
"v_rcp_f32 v98, v98\n"
"s_nop 0\n"
"v_mul_f32 v90, v90, v98\n"
"v_mul_f32 v91, v91, v98\n"
"v_mul_f32 v92, v92, v98\n"
"v_mul_f32 v93, v93, v98\n"
"v_mul_f32 v94, v94, v98\n"
"v_mul_f32 v95, v95, v98\n"
"v_mul_f32 v96, v96, v98\n"
"v_mul_f32 v97, v97, v98\n"
"v_cvt_pk_bf16_f32 v58, v90, v91\n"
"v_cvt_pk_bf16_f32 v59, v92, v93\n"
"v_cvt_pk_bf16_f32 v60, v94, v95\n"
"v_cvt_pk_bf16_f32 v61, v96, v97\n"
"ds_write_b128 v21, v[58:61] offset:0\n"
"s_waitcnt lgkmcnt(5)\n"
"v_permlane16_swap_b32 v62, v64\n"
"v_permlane16_swap_b32 v63, v65\n"
"s_nop 0\n"
"s_waitcnt vmcnt(4)\n"
"v_mfma_f32_32x32x16_bf16 v[90:105], v[62:65], a[140:143], 0\n"
"v_add_f32 v114, 897988541, v114\n"
"v_rcp_f32 v114, v114\n"
"s_nop 0\n"
"v_mul_f32 v106, v106, v114\n"
"v_mul_f32 v107, v107, v114\n"
"v_mul_f32 v108, v108, v114\n"
"v_mul_f32 v109, v109, v114\n"
"v_mul_f32 v110, v110, v114\n"
"v_mul_f32 v111, v111, v114\n"
"v_mul_f32 v112, v112, v114\n"
"v_mul_f32 v113, v113, v114\n"
"v_cvt_pk_bf16_f32 v138, v106, v107\n"
"v_cvt_pk_bf16_f32 v139, v108, v109\n"
"v_cvt_pk_bf16_f32 v140, v110, v111\n"
"v_cvt_pk_bf16_f32 v141, v112, v113\n"
"ds_write_b128 v21, v[138:141] offset:1024\n"
"s_waitcnt lgkmcnt(5)\n"
"v_permlane16_swap_b32 v66, v68\n"
"v_permlane16_swap_b32 v67, v69\n"
"s_nop 0\n"
"s_waitcnt vmcnt(3)\n"
"v_mfma_f32_32x32x16_bf16 v[106:121], v[66:69], a[144:147], 0\n"
"v_add_f32 v130, 897988541, v130\n"
"v_rcp_f32 v130, v130\n"
"s_nop 0\n"
"v_mul_f32 v122, v122, v130\n"
"v_mul_f32 v123, v123, v130\n"
"v_mul_f32 v124, v124, v130\n"
"v_mul_f32 v125, v125, v130\n"
"v_mul_f32 v126, v126, v130\n"
"v_mul_f32 v127, v127, v130\n"
"v_mul_f32 v128, v128, v130\n"
"v_mul_f32 v129, v129, v130\n"
"v_cvt_pk_bf16_f32 v58, v122, v123\n"
"v_cvt_pk_bf16_f32 v59, v124, v125\n"
"v_cvt_pk_bf16_f32 v60, v126, v127\n"
"v_cvt_pk_bf16_f32 v61, v128, v129\n"
"ds_write_b128 v21, v[58:61] offset:2048\n"
"s_waitcnt lgkmcnt(5)\n"
"v_permlane16_swap_b32 v70, v72\n"
"v_permlane16_swap_b32 v71, v73\n"
"s_nop 0\n"
"s_waitcnt vmcnt(2)\n"
"v_mfma_f32_32x32x16_bf16 v[122:137], v[70:73], a[148:151], 0\n"
"v_add_f32 v98, 897988541, v98\n"
"v_rcp_f32 v98, v98\n"
"s_nop 0\n"
"v_mul_f32 v90, v90, v98\n"
"v_mul_f32 v91, v91, v98\n"
"v_mul_f32 v92, v92, v98\n"
"v_mul_f32 v93, v93, v98\n"
"v_mul_f32 v94, v94, v98\n"
"v_mul_f32 v95, v95, v98\n"
"v_mul_f32 v96, v96, v98\n"
"v_mul_f32 v97, v97, v98\n"
"v_cvt_pk_bf16_f32 v138, v90, v91\n"
"v_cvt_pk_bf16_f32 v139, v92, v93\n"
"v_cvt_pk_bf16_f32 v140, v94, v95\n"
"v_cvt_pk_bf16_f32 v141, v96, v97\n"
"ds_write_b128 v21, v[138:141] offset:3072\n"
"s_waitcnt lgkmcnt(5)\n"
"v_permlane16_swap_b32 v78, v80\n"
"v_permlane16_swap_b32 v79, v81\n"
"s_nop 0\n"
"s_waitcnt vmcnt(1)\n"
"v_mfma_f32_32x32x16_bf16 v[90:105], v[78:81], a[152:155], 0\n"
"v_add_f32 v114, 897988541, v114\n"
"v_rcp_f32 v114, v114\n"
"s_nop 0\n"
"v_mul_f32 v106, v106, v114\n"
"v_mul_f32 v107, v107, v114\n"
"v_mul_f32 v108, v108, v114\n"
"v_mul_f32 v109, v109, v114\n"
"v_mul_f32 v110, v110, v114\n"
"v_mul_f32 v111, v111, v114\n"
"v_mul_f32 v112, v112, v114\n"
"v_mul_f32 v113, v113, v114\n"
"v_cvt_pk_bf16_f32 v58, v106, v107\n"
"v_cvt_pk_bf16_f32 v59, v108, v109\n"
"v_cvt_pk_bf16_f32 v60, v110, v111\n"
"v_cvt_pk_bf16_f32 v61, v112, v113\n"
"ds_write_b128 v22, v[58:61] offset:0\n"
"s_waitcnt lgkmcnt(5)\n"
"v_permlane16_swap_b32 v82, v84\n"
"v_permlane16_swap_b32 v83, v85\n"
"s_nop 0\n"
"s_waitcnt vmcnt(0)\n"
"v_mfma_f32_32x32x16_bf16 v[106:121], v[82:85], a[156:159], 0\n"
"v_add_f32 v130, 897988541, v130\n"
"v_rcp_f32 v130, v130\n"
"s_nop 0\n"
"v_mul_f32 v122, v122, v130\n"
"v_mul_f32 v123, v123, v130\n"
"v_mul_f32 v124, v124, v130\n"
"v_mul_f32 v125, v125, v130\n"
"v_mul_f32 v126, v126, v130\n"
"v_mul_f32 v127, v127, v130\n"
"v_mul_f32 v128, v128, v130\n"
"v_mul_f32 v129, v129, v130\n"
"v_cvt_pk_bf16_f32 v138, v122, v123\n"
"v_cvt_pk_bf16_f32 v139, v124, v125\n"
"v_cvt_pk_bf16_f32 v140, v126, v127\n"
"v_cvt_pk_bf16_f32 v141, v128, v129\n"
"ds_write_b128 v22, v[138:141] offset:1024\n"
"v_add_f32 v98, 897988541, v98\n"
"v_rcp_f32 v98, v98\n"
"s_nop 0\n"
"v_mul_f32 v90, v90, v98\n"
"v_mul_f32 v91, v91, v98\n"
"v_mul_f32 v92, v92, v98\n"
"v_mul_f32 v93, v93, v98\n"
"v_mul_f32 v94, v94, v98\n"
"v_mul_f32 v95, v95, v98\n"
"v_mul_f32 v96, v96, v98\n"
"v_mul_f32 v97, v97, v98\n"
"v_cvt_pk_bf16_f32 v58, v90, v91\n"
"v_cvt_pk_bf16_f32 v59, v92, v93\n"
"v_cvt_pk_bf16_f32 v60, v94, v95\n"
"v_cvt_pk_bf16_f32 v61, v96, v97\n"
"ds_write_b128 v22, v[58:61] offset:2048\n"
"v_add_f32 v114, 897988541, v114\n"
"v_rcp_f32 v114, v114\n"
"s_nop 0\n"
"v_mul_f32 v106, v106, v114\n"
"v_mul_f32 v107, v107, v114\n"
"v_mul_f32 v108, v108, v114\n"
"v_mul_f32 v109, v109, v114\n"
"v_mul_f32 v110, v110, v114\n"
"v_mul_f32 v111, v111, v114\n"
"v_mul_f32 v112, v112, v114\n"
"v_mul_f32 v113, v113, v114\n"
"v_cvt_pk_bf16_f32 v138, v106, v107\n"
"v_cvt_pk_bf16_f32 v139, v108, v109\n"
"v_cvt_pk_bf16_f32 v140, v110, v111\n"
"v_cvt_pk_bf16_f32 v141, v112, v113\n"
"ds_write_b128 v22, v[138:141] offset:3072\n"
"s_waitcnt lgkmcnt(0)\n"
"s_barrier\n"
"buffer_store_dwordx2 v[76:77], v5, s[8:11], 0 offen sc1\n"
"ds_read_b128 v[14:17], v23 offset:0\n"
"ds_read_b128 v[42:45], v23 offset:1024\n"
"ds_read_b128 v[46:49], v23 offset:2048\n"
"ds_read_b128 v[54:57], v23 offset:3072\n"
"s_waitcnt lgkmcnt(3)\n"
"v_mfma_f32_32x32x16_bf16 v[26:41], a[0:3], v[14:17], v[26:41]\n"
"ds_read_b128 v[58:61], v23 offset:4096\n"
"s_lshr_b32 s40, s37, 1\n"
"s_lshr_b32 s41, s36, 3\n"
"s_lshl2_add_u32 s26, s40, s41\n"
"s_and_b32 s40, s37, 1\n"
"s_waitcnt lgkmcnt(3)\n"
"v_mfma_f32_32x32x16_bf16 v[26:41], a[4:7], v[42:45], v[26:41]\n"
"ds_read_b128 v[62:65], v23 offset:5120\n"
"s_and_b32 s41, s36, 7\n"
"s_lshl3_add_u32 s44, s40, s41\n"
"s_lshr_b32 s40, s36, 3\n"
"s_lshl2_add_u32 s40, s40, s41\n"
"s_waitcnt lgkmcnt(3)\n"
"v_mfma_f32_32x32x16_bf16 v[26:41], a[8:11], v[46:49], v[26:41]\n"
"ds_read_b128 v[14:17], v23 offset:6144\n"
"s_lshl_b32 s47, s40, 1\n"
"s_and_b32 s47, s47, 31\n"
"s_lshr_b32 s45, s44, 2\n"
"s_and_b32 s46, s44, 3\n"
"s_waitcnt lgkmcnt(3)\n"
"v_mfma_f32_32x32x16_bf16 v[26:41], a[12:15], v[54:57], v[26:41]\n"
"ds_read_b128 v[42:45], v23 offset:7168\n"
"s_lshl_b32 s50, s15, 10\n"
"s_mul_i32 s40, s45, 7\n"
"s_lshl1_add_u32 s40, s46, s40\n"
"s_lshl_b32 s40, s40, 7\n"
"s_waitcnt lgkmcnt(3)\n"
"v_mfma_f32_32x32x16_bf16 v[26:41], a[16:19], v[58:61], v[26:41]\n"
"ds_read_b128 v[46:49], v23 offset:8192\n"
"s_mul_i32 s41, s27, 28672\n"
"s_add_u32 s49, s40, s41\n"
"s_mul_i32 s48, s47, 57344\n"
"v_lshrrev_b32 v5, 3, v1\n"
"s_waitcnt lgkmcnt(3)\n"
"v_mfma_f32_32x32x16_bf16 v[26:41], a[20:23], v[62:65], v[26:41]\n"
"ds_read_b128 v[54:57], v23 offset:9216\n"
"v_bfe_u32 v6, v1, 4, 1\n"
"s_lshl_b32 s40, s60, 1\n"
"v_or_b32 v6, s40, v6\n"
"v_lshlrev_b32 v6, 5, v6\n"
"s_waitcnt lgkmcnt(3)\n"
"v_mfma_f32_32x32x16_bf16 v[26:41], a[24:27], v[14:17], v[26:41]\n"
"ds_read_b128 v[58:61], v23 offset:10240\n"
"v_and_b32 v7, 0x70, v4\n"
"v_xor_b32 v6, v6, v7\n"
"v_mul_u32_u24 v25, 3584, v5\n"
"v_add3_u32 v25, v25, v6, s49\n"
"s_waitcnt lgkmcnt(3)\n"
"v_mfma_f32_32x32x16_bf16 v[26:41], a[28:31], v[42:45], v[26:41]\n"
"ds_read_b128 v[62:65], v23 offset:11264\n"
"s_waitcnt lgkmcnt(3)\n"
"v_mfma_f32_32x32x16_bf16 v[26:41], a[32:35], v[46:49], v[26:41]\n"
"ds_read_b128 v[14:17], v23 offset:12288\n"
"s_waitcnt lgkmcnt(3)\n"
"v_mfma_f32_32x32x16_bf16 v[26:41], a[36:39], v[54:57], v[26:41]\n"
"ds_read_b128 v[42:45], v23 offset:13312\n"
"s_waitcnt lgkmcnt(3)\n"
"v_mfma_f32_32x32x16_bf16 v[26:41], a[40:43], v[58:61], v[26:41]\n"
"ds_read_b128 v[46:49], v23 offset:14336\n"
"s_waitcnt lgkmcnt(3)\n"
"v_mfma_f32_32x32x16_bf16 v[26:41], a[44:47], v[62:65], v[26:41]\n"
"ds_read_b128 v[54:57], v23 offset:15360\n"
"s_waitcnt lgkmcnt(3)\n"
"v_mfma_f32_32x32x16_bf16 v[26:41], a[48:51], v[14:17], v[26:41]\n"
"ds_read_b128 v[58:61], v23 offset:16384\n"
"s_waitcnt lgkmcnt(3)\n"
"v_mfma_f32_32x32x16_bf16 v[26:41], a[52:55], v[42:45], v[26:41]\n"
"ds_read_b128 v[62:65], v23 offset:17408\n"
"s_waitcnt lgkmcnt(3)\n"
"v_mfma_f32_32x32x16_bf16 v[26:41], a[56:59], v[46:49], v[26:41]\n"
"ds_read_b128 v[14:17], v23 offset:18432\n"
"s_waitcnt lgkmcnt(3)\n"
"v_mfma_f32_32x32x16_bf16 v[26:41], a[60:63], v[54:57], v[26:41]\n"
"ds_read_b128 v[42:45], v23 offset:19456\n"
"s_waitcnt lgkmcnt(3)\n"
"v_mfma_f32_32x32x16_bf16 v[26:41], a[64:67], v[58:61], v[26:41]\n"
"ds_read_b128 v[46:49], v23 offset:20480\n"
"s_add_u32 m0, s50, 0\n"
"s_nop 0\n"
"buffer_load_dwordx4 v25, s[52:55], s48 offen lds\n"
"s_add_u32 s48, s48, 57344\n"
"s_cmp_eq_u32 s48, 1835008\n"
"s_cselect_b32 s48, 0, s48\n"
"s_waitcnt lgkmcnt(3)\n"
"v_mfma_f32_32x32x16_bf16 v[26:41], a[68:71], v[62:65], v[26:41]\n"
"ds_read_b128 v[54:57], v23 offset:21504\n"
"s_waitcnt lgkmcnt(3)\n"
"v_mfma_f32_32x32x16_bf16 v[26:41], a[72:75], v[14:17], v[26:41]\n"
"ds_read_b128 v[58:61], v23 offset:22528\n"
"s_add_u32 m0, s50, 4096\n"
"s_nop 0\n"
"buffer_load_dwordx4 v25, s[52:55], s48 offen lds\n"
"s_add_u32 s48, s48, 57344\n"
"s_cmp_eq_u32 s48, 1835008\n"
"s_cselect_b32 s48, 0, s48\n"
"s_waitcnt lgkmcnt(3)\n"
"v_mfma_f32_32x32x16_bf16 v[26:41], a[76:79], v[42:45], v[26:41]\n"
"ds_read_b128 v[62:65], v23 offset:23552\n"
"s_waitcnt lgkmcnt(3)\n"
"v_mfma_f32_32x32x16_bf16 v[26:41], a[80:83], v[46:49], v[26:41]\n"
"ds_read_b128 v[14:17], v23 offset:24576\n"
"s_add_u32 m0, s50, 8192\n"
"s_nop 0\n"
"buffer_load_dwordx4 v25, s[52:55], s48 offen lds\n"
"s_add_u32 s48, s48, 57344\n"
"s_cmp_eq_u32 s48, 1835008\n"
"s_cselect_b32 s48, 0, s48\n"
"s_waitcnt lgkmcnt(3)\n"
"v_mfma_f32_32x32x16_bf16 v[26:41], a[84:87], v[54:57], v[26:41]\n"
"ds_read_b128 v[42:45], v23 offset:25600\n"
"s_waitcnt lgkmcnt(3)\n"
"v_mfma_f32_32x32x16_bf16 v[26:41], a[88:91], v[58:61], v[26:41]\n"
"ds_read_b128 v[46:49], v23 offset:26624\n"
"s_add_u32 m0, s50, 12288\n"
"s_nop 0\n"
"buffer_load_dwordx4 v25, s[52:55], s48 offen lds\n"
"s_add_u32 s48, s48, 57344\n"
"s_cmp_eq_u32 s48, 1835008\n"
"s_cselect_b32 s48, 0, s48\n"
"s_waitcnt lgkmcnt(3)\n"
"v_mfma_f32_32x32x16_bf16 v[26:41], a[92:95], v[62:65], v[26:41]\n"
"ds_read_b128 v[54:57], v23 offset:27648\n"
"s_waitcnt lgkmcnt(3)\n"
"v_mfma_f32_32x32x16_bf16 v[26:41], a[96:99], v[14:17], v[26:41]\n"
"ds_read_b128 v[58:61], v23 offset:28672\n"
"s_add_u32 m0, s50, 16384\n"
"s_nop 0\n"
"buffer_load_dwordx4 v25, s[52:55], s48 offen lds\n"
"s_add_u32 s48, s48, 57344\n"
"s_cmp_eq_u32 s48, 1835008\n"
"s_cselect_b32 s48, 0, s48\n"
"s_waitcnt lgkmcnt(3)\n"
"v_mfma_f32_32x32x16_bf16 v[26:41], a[100:103], v[42:45], v[26:41]\n"
"ds_read_b128 v[62:65], v23 offset:29696\n"
"s_waitcnt lgkmcnt(3)\n"
"v_mfma_f32_32x32x16_bf16 v[26:41], a[104:107], v[46:49], v[26:41]\n"
"ds_read_b128 v[14:17], v23 offset:30720\n"
"s_add_u32 m0, s50, 20480\n"
"s_nop 0\n"
"buffer_load_dwordx4 v25, s[52:55], s48 offen lds\n"
"s_add_u32 s48, s48, 57344\n"
"s_cmp_eq_u32 s48, 1835008\n"
"s_cselect_b32 s48, 0, s48\n"
"s_waitcnt lgkmcnt(3)\n"
"v_mfma_f32_32x32x16_bf16 v[26:41], a[108:111], v[54:57], v[26:41]\n"
"ds_read_b128 v[42:45], v23 offset:31744\n"
"s_waitcnt lgkmcnt(3)\n"
"v_mfma_f32_32x32x16_bf16 v[26:41], a[112:115], v[58:61], v[26:41]\n"
"s_add_u32 m0, s50, 24576\n"
"s_nop 0\n"
"buffer_load_dwordx4 v25, s[52:55], s48 offen lds\n"
"s_add_u32 s48, s48, 57344\n"
"s_cmp_eq_u32 s48, 1835008\n"
"s_cselect_b32 s48, 0, s48\n"
"s_waitcnt lgkmcnt(2)\n"
"v_mfma_f32_32x32x16_bf16 v[26:41], a[116:119], v[62:65], v[26:41]\n"
"s_waitcnt lgkmcnt(1)\n"
"v_mfma_f32_32x32x16_bf16 v[26:41], a[120:123], v[14:17], v[26:41]\n"
"s_add_u32 m0, s50, 28672\n"
"s_nop 0\n"
"buffer_load_dwordx4 v25, s[52:55], s48 offen lds\n"
"s_add_u32 s48, s48, 57344\n"
"s_cmp_eq_u32 s48, 1835008\n"
"s_cselect_b32 s48, 0, s48\n"
"s_waitcnt lgkmcnt(0)\n"
"v_mfma_f32_32x32x16_bf16 v[26:41], a[124:127], v[42:45], v[26:41]\n"
"s_nop 11\n"
"v_cvt_pk_bf16_f32 v66, v26, v27\n"
"v_cvt_pk_bf16_f32 v67, v28, v29\n"
"v_cvt_pk_bf16_f32 v68, v30, v31\n"
"v_cvt_pk_bf16_f32 v69, v32, v33\n"
"buffer_store_dwordx4 v[66:69], v4, s[8:11], s18 offen offset:0 sc1\n"
"v_cvt_pk_bf16_f32 v70, v34, v35\n"
"v_cvt_pk_bf16_f32 v71, v36, v37\n"
"v_cvt_pk_bf16_f32 v72, v38, v39\n"
"v_cvt_pk_bf16_f32 v73, v40, v41\n"
"buffer_store_dwordx4 v[70:73], v4, s[8:11], s18 offen offset:1024 sc1\n"
"v_mov_b32 v20, 0\n"
"v_mov_b32 v21, 0\n"
"s_mov_b32 s40, 0x3f803f80\n"
"v_dot2c_f32_bf16 v20, s40, v66\n"
"v_dot2c_f32_bf16 v21, v66, v66\n"
"v_dot2c_f32_bf16 v20, s40, v67\n"
"v_dot2c_f32_bf16 v21, v67, v67\n"
"v_dot2c_f32_bf16 v20, s40, v68\n"
"v_dot2c_f32_bf16 v21, v68, v68\n"
"v_dot2c_f32_bf16 v20, s40, v69\n"
"v_dot2c_f32_bf16 v21, v69, v69\n"
"v_dot2c_f32_bf16 v20, s40, v70\n"
"v_dot2c_f32_bf16 v21, v70, v70\n"
"v_dot2c_f32_bf16 v20, s40, v71\n"
"v_dot2c_f32_bf16 v21, v71, v71\n"
"v_dot2c_f32_bf16 v20, s40, v72\n"
"v_dot2c_f32_bf16 v21, v72, v72\n"
"v_dot2c_f32_bf16 v20, s40, v73\n"
"v_dot2c_f32_bf16 v21, v73, v73\n"
"s_nop 1\n"
"v_mov_b32 v5, v20\n"
"v_mov_b32 v6, v21\n"
"s_nop 0\n"
"v_permlane32_swap_b32 v20, v5\n"
"v_permlane32_swap_b32 v21, v6\n"
"v_add_f32 v20, v20, v5\n"
"v_add_f32 v21, v21, v6\n"
"buffer_store_dwordx2 v[20:21], v24, s[8:11], s19 offen sc1\n"
"s_waitcnt lgkmcnt(0)\n"
"s_barrier\n"
"s_waitcnt vmcnt(0)\n"
"buffer_store_dwordx2 v[76:77], v53, s[8:11], s24 offen sc1\n"
"s_add_u32 m0, s50, 32768\n"
"s_nop 0\n"
"buffer_load_dwordx4 v25, s[52:55], s48 offen lds\n"
"s_add_u32 s48, s48, 57344\n"
"s_cmp_eq_u32 s48, 1835008\n"
"s_cselect_b32 s48, 0, s48\n"
"s_add_u32 m0, s50, 36864\n"
"s_nop 0\n"
"buffer_load_dwordx4 v25, s[52:55], s48 offen lds\n"
"s_add_u32 s48, s48, 57344\n"
"s_cmp_eq_u32 s48, 1835008\n"
"s_cselect_b32 s48, 0, s48\n"
"s_add_u32 m0, s50, 40960\n"
"s_nop 0\n"
"buffer_load_dwordx4 v25, s[52:55], s48 offen lds\n"
"s_add_u32 s48, s48, 57344\n"
"s_cmp_eq_u32 s48, 1835008\n"
"s_cselect_b32 s48, 0, s48\n"
"s_add_u32 m0, s50, 45056\n"
"s_nop 0\n"
"buffer_load_dwordx4 v25, s[52:55], s48 offen lds\n"
"s_add_u32 s48, s48, 57344\n"
"s_cmp_eq_u32 s48, 1835008\n"
"s_cselect_b32 s48, 0, s48\n"
"s_add_u32 m0, s50, 49152\n"
"s_nop 0\n"
"buffer_load_dwordx4 v25, s[52:55], s48 offen lds\n"
"s_add_u32 s48, s48, 57344\n"
"s_cmp_eq_u32 s48, 1835008\n"
"s_cselect_b32 s48, 0, s48\n"
"s_add_u32 m0, s50, 53248\n"
"s_nop 0\n"
"buffer_load_dwordx4 v25, s[52:55], s48 offen lds\n"
"s_add_u32 s48, s48, 57344\n"
"s_cmp_eq_u32 s48, 1835008\n"
"s_cselect_b32 s48, 0, s48\n"
"s_add_u32 m0, s50, 57344\n"
"s_nop 0\n"
"buffer_load_dwordx4 v25, s[52:55], s48 offen lds\n"
"s_add_u32 s48, s48, 57344\n"
"s_cmp_eq_u32 s48, 1835008\n"
"s_cselect_b32 s48, 0, s48\n"
"s_add_u32 m0, s50, 61440\n"
"s_nop 0\n"
"buffer_load_dwordx4 v25, s[52:55], s48 offen lds\n"
"s_add_u32 s48, s48, 57344\n"
"s_cmp_eq_u32 s48, 1835008\n"
"s_cselect_b32 s48, 0, s48\n"
"s_add_u32 m0, s50, 65536\n"
"s_nop 0\n"
"buffer_load_dwordx4 v25, s[52:55], s48 offen lds\n"
"s_add_u32 s48, s48, 57344\n"
"s_cmp_eq_u32 s48, 1835008\n"
"s_cselect_b32 s48, 0, s48\n"
"s_add_u32 m0, s50, 69632\n"
"s_nop 0\n"
"buffer_load_dwordx4 v25, s[52:55], s48 offen lds\n"
"s_add_u32 s48, s48, 57344\n"
"s_cmp_eq_u32 s48, 1835008\n"
"s_cselect_b32 s48, 0, s48\n"
"s_add_u32 m0, s50, 73728\n"
"s_nop 0\n"
"buffer_load_dwordx4 v25, s[52:55], s48 offen lds\n"
"s_add_u32 s48, s48, 57344\n"
"s_cmp_eq_u32 s48, 1835008\n"
"s_cselect_b32 s48, 0, s48\n"
"s_add_u32 m0, s50, 77824\n"
"s_nop 0\n"
"buffer_load_dwordx4 v25, s[52:55], s48 offen lds\n"
"s_add_u32 s48, s48, 57344\n"
"s_cmp_eq_u32 s48, 1835008\n"
"s_cselect_b32 s48, 0, s48\n"
"s_add_u32 m0, s50, 81920\n"
"s_nop 0\n"
"buffer_load_dwordx4 v25, s[52:55], s48 offen lds\n"
"s_add_u32 s48, s48, 57344\n"
"s_cmp_eq_u32 s48, 1835008\n"
"s_cselect_b32 s48, 0, s48\n"
"s_add_u32 m0, s50, 86016\n"
"s_nop 0\n"
"buffer_load_dwordx4 v25, s[52:55], s48 offen lds\n"
"s_add_u32 s48, s48, 57344\n"
"s_cmp_eq_u32 s48, 1835008\n"
"s_cselect_b32 s48, 0, s48\n"
"s_add_u32 m0, s50, 90112\n"
"s_nop 0\n"
"buffer_load_dwordx4 v25, s[52:55], s48 offen lds\n"
"s_add_u32 s48, s48, 57344\n"
"s_cmp_eq_u32 s48, 1835008\n"
"s_cselect_b32 s48, 0, s48\n"
"s_add_u32 m0, s50, 94208\n"
"s_nop 0\n"
"buffer_load_dwordx4 v25, s[52:55], s48 offen lds\n"
"s_add_u32 s48, s48, 57344\n"
"s_cmp_eq_u32 s48, 1835008\n"
"s_cselect_b32 s48, 0, s48\n"
"s_add_u32 m0, s50, 98304\n"
"s_nop 0\n"
"buffer_load_dwordx4 v25, s[52:55], s48 offen lds\n"
"s_add_u32 s48, s48, 57344\n"
"s_cmp_eq_u32 s48, 1835008\n"
"s_cselect_b32 s48, 0, s48\n"
"s_add_u32 m0, s50, 102400\n"
"s_nop 0\n"
"buffer_load_dwordx4 v25, s[52:55], s48 offen lds\n"
"s_add_u32 s48, s48, 57344\n"
"s_cmp_eq_u32 s48, 1835008\n"
"s_cselect_b32 s48, 0, s48\n"
"s_add_u32 m0, s50, 106496\n"
"s_nop 0\n"
"buffer_load_dwordx4 v25, s[52:55], s48 offen lds\n"
"s_add_u32 s48, s48, 57344\n"
"s_cmp_eq_u32 s48, 1835008\n"
"s_cselect_b32 s48, 0, s48\n"
"s_add_u32 m0, s50, 110592\n"
"s_nop 0\n"
"buffer_load_dwordx4 v25, s[52:55], s48 offen lds\n"
"s_add_u32 s48, s48, 57344\n"
"s_cmp_eq_u32 s48, 1835008\n"
"s_cselect_b32 s48, 0, s48\n"
"s_add_u32 m0, s50, 114688\n"
"s_nop 0\n"
"buffer_load_dwordx4 v25, s[52:55], s48 offen lds\n"
"s_add_u32 s48, s48, 57344\n"
"s_cmp_eq_u32 s48, 1835008\n"
"s_cselect_b32 s48, 0, s48\n"
"s_add_u32 m0, s50, 118784\n"
"s_nop 0\n"
"buffer_load_dwordx4 v25, s[52:55], s48 offen lds\n"
"s_add_u32 s48, s48, 57344\n"
"s_cmp_eq_u32 s48, 1835008\n"
"s_cselect_b32 s48, 0, s48\n"
"s_add_u32 m0, s50, 122880\n"
"s_nop 0\n"
"buffer_load_dwordx4 v25, s[52:55], s48 offen lds\n"
"s_add_u32 s48, s48, 57344\n"
"s_cmp_eq_u32 s48, 1835008\n"
"s_cselect_b32 s48, 0, s48\n"
"s_add_u32 m0, s50, 126976\n"
"s_nop 0\n"
"buffer_load_dwordx4 v25, s[52:55], s48 offen lds\n"
"s_add_u32 s48, s48, 57344\n"
"s_cmp_eq_u32 s48, 1835008\n"
"s_cselect_b32 s48, 0, s48\n"
"s_mul_i32 s40, s45, 896\n"
"s_add_u32 s40, s40, 128\n"
"s_mul_i32 s41, s46, 3\n"
"s_mov_b32 s42, 43691\n"
"v_add_u32 v5, 0, v1\n"
"v_mul_u32_u24 v6, s42, v5\n"
"v_lshrrev_b32 v6, 18, v6\n"
"v_mul_u32_u24 v7, 6, v6\n"
"v_sub_u32 v7, v5, v7\n"
"v_lshrrev_b32 v8, 1, v7\n"
"v_add_u32 v8, s41, v8\n"
"v_lshrrev_b32 v9, 2, v8\n"
"v_and_b32 v8, 3, v8\n"
"v_lshlrev_b32 v8, 5, v8\n"
"v_lshl_add_u32 v8, v9, 8, v8\n"
"v_and_b32 v7, 1, v7\n"
"v_lshl_add_u32 v8, v7, 4, v8\n"
"v_mul_u32_u24 v6, 3584, v6\n"
"v_add3_u32 v13, v6, v8, s40\n"
"v_add_u32 v5, 64, v1\n"
"v_mul_u32_u24 v6, s42, v5\n"
"v_lshrrev_b32 v6, 18, v6\n"
"v_mul_u32_u24 v7, 6, v6\n"
"v_sub_u32 v7, v5, v7\n"
"v_lshrrev_b32 v8, 1, v7\n"
"v_add_u32 v8, s41, v8\n"
"v_lshrrev_b32 v9, 2, v8\n"
"v_and_b32 v8, 3, v8\n"
"v_lshlrev_b32 v8, 5, v8\n"
"v_lshl_add_u32 v8, v9, 8, v8\n"
"v_and_b32 v7, 1, v7\n"
"v_lshl_add_u32 v8, v7, 4, v8\n"
"v_mul_u32_u24 v6, 3584, v6\n"
"v_add3_u32 v14, v6, v8, s40\n"
"v_subrev_u32 v14, 1024, v14\n"
"v_add_u32 v5, 128, v1\n"
"v_mul_u32_u24 v6, s42, v5\n"
"v_lshrrev_b32 v6, 18, v6\n"
"v_mul_u32_u24 v7, 6, v6\n"
"v_sub_u32 v7, v5, v7\n"
"v_lshrrev_b32 v8, 1, v7\n"
"v_add_u32 v8, s41, v8\n"
"v_lshrrev_b32 v9, 2, v8\n"
"v_and_b32 v8, 3, v8\n"
"v_lshlrev_b32 v8, 5, v8\n"
"v_lshl_add_u32 v8, v9, 8, v8\n"
"v_and_b32 v7, 1, v7\n"
"v_lshl_add_u32 v8, v7, 4, v8\n"
"v_mul_u32_u24 v6, 3584, v6\n"
"v_add3_u32 v15, v6, v8, s40\n"
"v_subrev_u32 v15, 2048, v15\n"
"s_lshl1_add_u32 s40, s27, s47\n"
"s_and_b32 s40, s40, 31\n"
"s_mul_i32 s4, s40, 57344\n"
"s_mul_i32 s41, s27, 3072\n"
"s_mul_i32 s40, s60, 12416\n"
"s_add_u32 s5, s40, s41\n"
"s_mul_i32 s40, s60, 36992\n"
"s_add_u32 s6, s40, s41\n"
"s_add_u32 m0, s5, 131072\n"
"s_nop 0\n"
"buffer_load_dwordx4 v13, s[52:55], s4 offen lds\n"
"buffer_load_dwordx4 v14, s[52:55], s4 offen offset:1024 lds\n"
"buffer_load_dwordx4 v15, s[52:55], s4 offen offset:2048 lds\n"
"s_add_u32 s4, s4, 229376\n"
"s_cmp_ge_u32 s4, 1835008\n"
"s_cselect_b32 s43, 1835008, 0\n"
"s_sub_u32 s4, s4, s43\n"
"s_add_u32 m0, s5, 137216\n"
"s_nop 0\n"
"buffer_load_dwordx4 v13, s[52:55], s4 offen lds\n"
"buffer_load_dwordx4 v14, s[52:55], s4 offen offset:1024 lds\n"
"buffer_load_dwordx4 v15, s[52:55], s4 offen offset:2048 lds\n"
"s_add_u32 s4, s4, 229376\n"
"s_cmp_ge_u32 s4, 1835008\n"
"s_cselect_b32 s43, 1835008, 0\n"
"s_sub_u32 s4, s4, s43\n"
"v_bfe_u32 v5, v1, 2, 2\n"
"v_lshl_add_u32 v6, v3, 2, v5\n"
"v_bfe_u32 v7, v1, 4, 1\n"
"v_and_b32 v8, 3, v1\n"
"v_lshlrev_b32 v8, 3, v8\n"
"v_lshl_add_u32 v9, v6, 7, v8\n"
"v_lshl_add_u32 v9, v7, 11, v9\n"
"v_lshrrev_b32 v5, 1, v5\n"
"v_lshl_add_u32 v5, v7, 1, v5\n"
"v_lshl_add_u32 v16, v5, 5, v9\n"
"v_xor_b32 v18, 32, v16\n"
"v_xor_b32 v21, 64, v16\n"
"v_xor_b32 v23, 96, v16\n"
"v_add_u32 v17, 0x10000, v16\n"
"v_add_u32 v20, 0x10000, v18\n"
"v_add_u32 v22, 0x10000, v21\n"
"v_add_u32 v26, 0x10000, v23\n"
"s_lshl_b32 s40, s15, 5\n"
"v_xor_b32 v29, s40, v16\n"
"v_add_u32 v30, 0x10000, v29\n"
"v_mul_u32_u24 v9, 96, v6\n"
"v_add_u32 v9, v9, v8\n"
"v_mul_u32_u24 v5, 12416, v7\n"
"s_mov_b32 s40, 131072\n"
"v_add3_u32 v27, v5, v9, s40\n"
"v_mul_u32_u24 v5, 36992, v7\n"
"v_add_u32 v28, v5, v9\n"
"s_min_u32 s40, s15, 2\n"
"s_lshl_b32 s40, s40, 5\n"
"v_add_u32 v31, s40, v27\n"
"v_add_u32 v32, s40, v28\n"
"s_lshl2_add_u32 s40, s26, s15\n"
"s_lshl_b32 s41, s40, 15\n"
"s_add_u32 s16, s41, 6291456\n"
"s_lshl_b32 s7, s47, 10\n"
"s_lshl_b32 s41, s40, 11\n"
"s_add_u32 s17, s41, 135168\n"
"s_lshl_b32 s41, s40, 12\n"
"s_add_u32 s18, s41, 8388608\n"
"v_and_b32 v33, 15, v1\n"
"v_lshlrev_b32 v33, 7, v33\n"
"v_add_u32 v38, s16, v4\n"
"v_lshlrev_b32 v37, 2, v1\n"
"v_add_u32 v37, 159744, v37\n"
"s_lshl_b32 s40, s15, 8\n"
"v_add_u32 v36, s40, v37\n"
"s_lshl2_add_u32 s40, s26, s15\n"
"s_mul_i32 s40, s40, 114688\n"
"s_add_u32 s40, s40, 9437184\n"
"s_mul_i32 s41, s45, 28672\n"
"s_add_u32 s40, s40, s41\n"
"s_lshl_b32 s41, s46, 13\n"
"s_add_u32 s19, s40, s41\n"
"s_add_u32 s40, s40, 4096\n"
"s_mul_i32 s41, s46, 3\n"
"s_add_u32 s41, s41, 0\n"
"s_lshr_b32 s42, s41, 2\n"
"s_lshl2_add_u32 s41, s42, s41\n"
"s_lshl_b32 s41, s41, 10\n"
"s_add_u32 s20, s40, s41\n"
"s_mul_i32 s41, s46, 3\n"
"s_add_u32 s41, s41, 1\n"
"s_lshr_b32 s42, s41, 2\n"
"s_lshl2_add_u32 s41, s42, s41\n"
"s_lshl_b32 s41, s41, 10\n"
"s_add_u32 s21, s40, s41\n"
"s_mul_i32 s41, s46, 3\n"
"s_add_u32 s41, s41, 2\n"
"s_lshr_b32 s42, s41, 2\n"
"s_lshl2_add_u32 s41, s42, s41\n"
"s_lshl_b32 s41, s41, 10\n"
"s_add_u32 s22, s40, s41\n"
"s_lshl4_add_u32 s40, s26, s44\n"
"s_lshl2_add_u32 s40, s40, s15\n"
"s_lshl_b32 s40, s40, 7\n"
"s_add_u32 s23, s40, 266240\n"
"s_load_dwordx2 s[56:57], %0, 40\n"
"s_mov_b32 s58, 1835008\n"
"s_mov_b32 s59, 0x20000\n"
"s_and_b32 s24, s36, 7\n"
"s_bfe_u32 s51, s36, 0x10003\n"
"s_lshr_b32 s40, s36, 4\n"
"s_lshl1_add_u32 s61, s37, s40\n"
"v_lshrrev_b32 v5, 3, v1\n"
"v_bfe_u32 v6, v1, 4, 1\n"
"v_lshlrev_b32 v6, 6, v6\n"
"v_and_b32 v7, 0x70, v4\n"
"v_xor_b32 v6, v6, v7\n"
"v_lshl_add_u32 v39, v5, 10, v6\n"
"v_add_u32 v40, 7168, v39\n"
"s_mul_i32 s40, s15, 458752\n"
"s_lshl_b32 s41, s24, 7\n"
"s_add_u32 s65, s40, s41\n"
"s_mul_i32 s40, s15, 12288\n"
"s_add_u32 s66, s40, 77824\n"
"s_mul_i32 s40, s15, 6144\n"
"s_add_u32 s67, s40, 131072\n"
"s_mul_i32 s40, s60, 36992\n"
"s_mul_i32 s41, s27, 6144\n"
"s_add_u32 s68, s40, s41\n"
"v_mov_b32 v42, 0x3f803f80\n"
"v_mov_b32 v43, 0x3f803f80\n"
"s_waitcnt vmcnt(6)\n"
"s_waitcnt lgkmcnt(0)\n"
"s_barrier\n"
"ds_read_b64_tr_b16 v[44:45], v29 offset:0\n"
"ds_read_b64_tr_b16 v[46:47], v29 offset:1024\n"
"ds_read_b64_tr_b16 v[48:49], v29 offset:4096\n"
"ds_read_b64_tr_b16 v[50:51], v29 offset:5120\n"
"ds_read_b64_tr_b16 v[54:55], v29 offset:8192\n"
"ds_read_b64_tr_b16 v[56:57], v29 offset:9216\n"
"ds_read_b64_tr_b16 v[58:59], v29 offset:12288\n"
"ds_read_b64_tr_b16 v[60:61], v29 offset:13312\n"
"ds_read_b64_tr_b16 v[62:63], v29 offset:16384\n"
"ds_read_b64_tr_b16 v[64:65], v29 offset:17408\n"
"s_waitcnt lgkmcnt(9)\n"
"v_mfma_f32_4x4x4_16b_bf16 v[82:85], v[42:43], v[44:45], 0\n"
"s_waitcnt lgkmcnt(8)\n"
"v_mfma_f32_4x4x4_16b_bf16 v[86:89], v[42:43], v[46:47], 0\n"
"ds_read_b64_tr_b16 v[66:67], v29 offset:20480\n"
"ds_read_b64_tr_b16 v[68:69], v29 offset:21504\n"
"s_waitcnt lgkmcnt(9)\n"
"v_mfma_f32_4x4x4_16b_bf16 v[82:85], v[42:43], v[48:49], v[82:85]\n"
"s_waitcnt lgkmcnt(8)\n"
"v_mfma_f32_4x4x4_16b_bf16 v[86:89], v[42:43], v[50:51], v[86:89]\n"
"ds_read_b64_tr_b16 v[70:71], v29 offset:24576\n"
"ds_read_b64_tr_b16 v[72:73], v29 offset:25600\n"
"s_waitcnt lgkmcnt(9)\n"
"v_mfma_f32_4x4x4_16b_bf16 v[82:85], v[42:43], v[54:55], v[82:85]\n"
"s_waitcnt lgkmcnt(8)\n"
"v_mfma_f32_4x4x4_16b_bf16 v[86:89], v[42:43], v[56:57], v[86:89]\n"
"ds_read_b64_tr_b16 v[78:79], v29 offset:28672\n"
"ds_read_b64_tr_b16 v[80:81], v29 offset:29696\n"
"s_waitcnt lgkmcnt(9)\n"
"v_mfma_f32_4x4x4_16b_bf16 v[82:85], v[42:43], v[58:59], v[82:85]\n"
"s_waitcnt lgkmcnt(8)\n"
"v_mfma_f32_4x4x4_16b_bf16 v[86:89], v[42:43], v[60:61], v[86:89]\n"
"ds_read_b64_tr_b16 v[44:45], v29 offset:32768\n"
"ds_read_b64_tr_b16 v[46:47], v29 offset:33792\n"
"s_waitcnt lgkmcnt(9)\n"
"v_mfma_f32_4x4x4_16b_bf16 v[82:85], v[42:43], v[62:63], v[82:85]\n"
"s_waitcnt lgkmcnt(8)\n"
"v_mfma_f32_4x4x4_16b_bf16 v[86:89], v[42:43], v[64:65], v[86:89]\n"
"ds_read_b64_tr_b16 v[48:49], v29 offset:36864\n"
"ds_read_b64_tr_b16 v[50:51], v29 offset:37888\n"
"s_waitcnt lgkmcnt(9)\n"
"v_mfma_f32_4x4x4_16b_bf16 v[82:85], v[42:43], v[66:67], v[82:85]\n"
"s_waitcnt lgkmcnt(8)\n"
"v_mfma_f32_4x4x4_16b_bf16 v[86:89], v[42:43], v[68:69], v[86:89]\n"
"ds_read_b64_tr_b16 v[54:55], v29 offset:40960\n"
"ds_read_b64_tr_b16 v[56:57], v29 offset:41984\n"
"s_waitcnt lgkmcnt(9)\n"
"v_mfma_f32_4x4x4_16b_bf16 v[82:85], v[42:43], v[70:71], v[82:85]\n"
"s_waitcnt lgkmcnt(8)\n"
"v_mfma_f32_4x4x4_16b_bf16 v[86:89], v[42:43], v[72:73], v[86:89]\n"
"ds_read_b64_tr_b16 v[58:59], v29 offset:45056\n"
"ds_read_b64_tr_b16 v[60:61], v29 offset:46080\n"
"s_waitcnt lgkmcnt(9)\n"
"v_mfma_f32_4x4x4_16b_bf16 v[82:85], v[42:43], v[78:79], v[82:85]\n"
"s_waitcnt lgkmcnt(8)\n"
"v_mfma_f32_4x4x4_16b_bf16 v[86:89], v[42:43], v[80:81], v[86:89]\n"
"ds_read_b64_tr_b16 v[62:63], v29 offset:49152\n"
"ds_read_b64_tr_b16 v[64:65], v29 offset:50176\n"
"s_waitcnt lgkmcnt(9)\n"
"v_mfma_f32_4x4x4_16b_bf16 v[82:85], v[42:43], v[44:45], v[82:85]\n"
"s_waitcnt lgkmcnt(8)\n"
"v_mfma_f32_4x4x4_16b_bf16 v[86:89], v[42:43], v[46:47], v[86:89]\n"
"ds_read_b64_tr_b16 v[66:67], v29 offset:53248\n"
"ds_read_b64_tr_b16 v[68:69], v29 offset:54272\n"
"s_waitcnt lgkmcnt(9)\n"
"v_mfma_f32_4x4x4_16b_bf16 v[82:85], v[42:43], v[48:49], v[82:85]\n"
"s_waitcnt lgkmcnt(8)\n"
"v_mfma_f32_4x4x4_16b_bf16 v[86:89], v[42:43], v[50:51], v[86:89]\n"
"ds_read_b64_tr_b16 v[70:71], v29 offset:57344\n"
"ds_read_b64_tr_b16 v[72:73], v29 offset:58368\n"
"s_waitcnt lgkmcnt(9)\n"
"v_mfma_f32_4x4x4_16b_bf16 v[82:85], v[42:43], v[54:55], v[82:85]\n"
"s_waitcnt lgkmcnt(8)\n"
"v_mfma_f32_4x4x4_16b_bf16 v[86:89], v[42:43], v[56:57], v[86:89]\n"
"ds_read_b64_tr_b16 v[78:79], v29 offset:61440\n"
"ds_read_b64_tr_b16 v[80:81], v29 offset:62464\n"
"s_waitcnt lgkmcnt(9)\n"
"v_mfma_f32_4x4x4_16b_bf16 v[82:85], v[42:43], v[58:59], v[82:85]\n"
"s_waitcnt lgkmcnt(8)\n"
"v_mfma_f32_4x4x4_16b_bf16 v[86:89], v[42:43], v[60:61], v[86:89]\n"
"ds_read_b64_tr_b16 v[44:45], v30 offset:0\n"
"ds_read_b64_tr_b16 v[46:47], v30 offset:1024\n"
"s_waitcnt lgkmcnt(9)\n"
"v_mfma_f32_4x4x4_16b_bf16 v[82:85], v[42:43], v[62:63], v[82:85]\n"
"s_waitcnt lgkmcnt(8)\n"
"v_mfma_f32_4x4x4_16b_bf16 v[86:89], v[42:43], v[64:65], v[86:89]\n"
"ds_read_b64_tr_b16 v[48:49], v30 offset:4096\n"
"ds_read_b64_tr_b16 v[50:51], v30 offset:5120\n"
"s_waitcnt lgkmcnt(9)\n"
"v_mfma_f32_4x4x4_16b_bf16 v[82:85], v[42:43], v[66:67], v[82:85]\n"
"s_waitcnt lgkmcnt(8)\n"
"v_mfma_f32_4x4x4_16b_bf16 v[86:89], v[42:43], v[68:69], v[86:89]\n"
"ds_read_b64_tr_b16 v[54:55], v30 offset:8192\n"
"ds_read_b64_tr_b16 v[56:57], v30 offset:9216\n"
"s_waitcnt lgkmcnt(9)\n"
"v_mfma_f32_4x4x4_16b_bf16 v[82:85], v[42:43], v[70:71], v[82:85]\n"
"s_waitcnt lgkmcnt(8)\n"
"v_mfma_f32_4x4x4_16b_bf16 v[86:89], v[42:43], v[72:73], v[86:89]\n"
"ds_read_b64_tr_b16 v[58:59], v30 offset:12288\n"
"ds_read_b64_tr_b16 v[60:61], v30 offset:13312\n"
"s_waitcnt lgkmcnt(9)\n"
"v_mfma_f32_4x4x4_16b_bf16 v[82:85], v[42:43], v[78:79], v[82:85]\n"
"s_waitcnt lgkmcnt(8)\n"
"v_mfma_f32_4x4x4_16b_bf16 v[86:89], v[42:43], v[80:81], v[86:89]\n"
"buffer_load_dwordx2 v[34:35], v33, s[8:11], s17 offen sc1\n"
"ds_read_b64_tr_b16 v[62:63], v30 offset:16384\n"
"ds_read_b64_tr_b16 v[64:65], v30 offset:17408\n"
"s_waitcnt lgkmcnt(9)\n"
"v_mfma_f32_4x4x4_16b_bf16 v[82:85], v[42:43], v[44:45], v[82:85]\n"
"s_waitcnt lgkmcnt(8)\n"
"v_mfma_f32_4x4x4_16b_bf16 v[86:89], v[42:43], v[46:47], v[86:89]\n"
"ds_read_b64_tr_b16 v[66:67], v30 offset:20480\n"
"ds_read_b64_tr_b16 v[68:69], v30 offset:21504\n"
"s_waitcnt lgkmcnt(9)\n"
"v_mfma_f32_4x4x4_16b_bf16 v[82:85], v[42:43], v[48:49], v[82:85]\n"
"s_waitcnt lgkmcnt(8)\n"
"v_mfma_f32_4x4x4_16b_bf16 v[86:89], v[42:43], v[50:51], v[86:89]\n"
"ds_read_b64_tr_b16 v[70:71], v30 offset:24576\n"
"ds_read_b64_tr_b16 v[72:73], v30 offset:25600\n"
"s_waitcnt lgkmcnt(9)\n"
"v_mfma_f32_4x4x4_16b_bf16 v[82:85], v[42:43], v[54:55], v[82:85]\n"
"s_waitcnt lgkmcnt(8)\n"
"v_mfma_f32_4x4x4_16b_bf16 v[86:89], v[42:43], v[56:57], v[86:89]\n"
"ds_read_b64_tr_b16 v[78:79], v30 offset:28672\n"
"ds_read_b64_tr_b16 v[80:81], v30 offset:29696\n"
"s_waitcnt lgkmcnt(9)\n"
"v_mfma_f32_4x4x4_16b_bf16 v[82:85], v[42:43], v[58:59], v[82:85]\n"
"s_waitcnt lgkmcnt(8)\n"
"v_mfma_f32_4x4x4_16b_bf16 v[86:89], v[42:43], v[60:61], v[86:89]\n"
"ds_read_b64_tr_b16 v[44:45], v30 offset:32768\n"
"ds_read_b64_tr_b16 v[46:47], v30 offset:33792\n"
"s_waitcnt lgkmcnt(9)\n"
"v_mfma_f32_4x4x4_16b_bf16 v[82:85], v[42:43], v[62:63], v[82:85]\n"
"s_waitcnt lgkmcnt(8)\n"
"v_mfma_f32_4x4x4_16b_bf16 v[86:89], v[42:43], v[64:65], v[86:89]\n"
"ds_read_b64_tr_b16 v[48:49], v30 offset:36864\n"
"ds_read_b64_tr_b16 v[50:51], v30 offset:37888\n"
"s_waitcnt lgkmcnt(9)\n"
"v_mfma_f32_4x4x4_16b_bf16 v[82:85], v[42:43], v[66:67], v[82:85]\n"
"s_waitcnt lgkmcnt(8)\n"
"v_mfma_f32_4x4x4_16b_bf16 v[86:89], v[42:43], v[68:69], v[86:89]\n"
"ds_read_b64_tr_b16 v[54:55], v30 offset:40960\n"
"ds_read_b64_tr_b16 v[56:57], v30 offset:41984\n"
"s_waitcnt lgkmcnt(9)\n"
"v_mfma_f32_4x4x4_16b_bf16 v[82:85], v[42:43], v[70:71], v[82:85]\n"
"s_waitcnt lgkmcnt(8)\n"
"v_mfma_f32_4x4x4_16b_bf16 v[86:89], v[42:43], v[72:73], v[86:89]\n"
"ds_read_b64_tr_b16 v[58:59], v30 offset:45056\n"
"ds_read_b64_tr_b16 v[60:61], v30 offset:46080\n"
"s_waitcnt lgkmcnt(9)\n"
"v_mfma_f32_4x4x4_16b_bf16 v[82:85], v[42:43], v[78:79], v[82:85]\n"
"s_waitcnt lgkmcnt(8)\n"
"v_mfma_f32_4x4x4_16b_bf16 v[86:89], v[42:43], v[80:81], v[86:89]\n"
"ds_read_b64_tr_b16 v[62:63], v30 offset:49152\n"
"ds_read_b64_tr_b16 v[64:65], v30 offset:50176\n"
"s_waitcnt lgkmcnt(9)\n"
"v_mfma_f32_4x4x4_16b_bf16 v[82:85], v[42:43], v[44:45], v[82:85]\n"
"s_waitcnt lgkmcnt(8)\n"
"v_mfma_f32_4x4x4_16b_bf16 v[86:89], v[42:43], v[46:47], v[86:89]\n"
"ds_read_b64_tr_b16 v[66:67], v30 offset:53248\n"
"ds_read_b64_tr_b16 v[68:69], v30 offset:54272\n"
"s_waitcnt lgkmcnt(9)\n"
"v_mfma_f32_4x4x4_16b_bf16 v[82:85], v[42:43], v[48:49], v[82:85]\n"
"s_waitcnt lgkmcnt(8)\n"
"v_mfma_f32_4x4x4_16b_bf16 v[86:89], v[42:43], v[50:51], v[86:89]\n"
"ds_read_b64_tr_b16 v[70:71], v30 offset:57344\n"
"ds_read_b64_tr_b16 v[72:73], v30 offset:58368\n"
"s_waitcnt lgkmcnt(9)\n"
"v_mfma_f32_4x4x4_16b_bf16 v[82:85], v[42:43], v[54:55], v[82:85]\n"
"s_waitcnt lgkmcnt(8)\n"
"v_mfma_f32_4x4x4_16b_bf16 v[86:89], v[42:43], v[56:57], v[86:89]\n"
"ds_read_b64_tr_b16 v[78:79], v30 offset:61440\n"
"ds_read_b64_tr_b16 v[80:81], v30 offset:62464\n"
"s_waitcnt lgkmcnt(9)\n"
"v_mfma_f32_4x4x4_16b_bf16 v[82:85], v[42:43], v[58:59], v[82:85]\n"
"s_waitcnt lgkmcnt(8)\n"
"v_mfma_f32_4x4x4_16b_bf16 v[86:89], v[42:43], v[60:61], v[86:89]\n"
"s_waitcnt lgkmcnt(7)\n"
"v_mfma_f32_4x4x4_16b_bf16 v[82:85], v[42:43], v[62:63], v[82:85]\n"
"s_waitcnt lgkmcnt(6)\n"
"v_mfma_f32_4x4x4_16b_bf16 v[86:89], v[42:43], v[64:65], v[86:89]\n"
"s_waitcnt lgkmcnt(5)\n"
"v_mfma_f32_4x4x4_16b_bf16 v[82:85], v[42:43], v[66:67], v[82:85]\n"
"s_waitcnt lgkmcnt(4)\n"
"v_mfma_f32_4x4x4_16b_bf16 v[86:89], v[42:43], v[68:69], v[86:89]\n"
"s_waitcnt lgkmcnt(3)\n"
"v_mfma_f32_4x4x4_16b_bf16 v[82:85], v[42:43], v[70:71], v[82:85]\n"
"s_waitcnt lgkmcnt(2)\n"
"v_mfma_f32_4x4x4_16b_bf16 v[86:89], v[42:43], v[72:73], v[86:89]\n"
"s_waitcnt lgkmcnt(1)\n"
"v_mfma_f32_4x4x4_16b_bf16 v[82:85], v[42:43], v[78:79], v[82:85]\n"
"s_waitcnt lgkmcnt(0)\n"
"v_mfma_f32_4x4x4_16b_bf16 v[86:89], v[42:43], v[80:81], v[86:89]\n"
"s_nop 4\n"
"v_add_f32 v82, v82, v86\n"
"ds_write_b32 v36, v82\n"
"s_waitcnt vmcnt(0)\n"
"v_cmp_ne_u64 vcc, s[12:13], v[34:35]\n"
"s_nop 4\n"
"s_cbranch_vccnz .Lretry4_%=\n"
".Lback4_%=:\n"
"buffer_load_dwordx4 a[0:3], v38, s[8:11], s7 offen sc1\n"
"s_add_u32 s7, s7, 1024\n"
"s_and_b32 s7, s7, 0x7c00\n"
"buffer_load_dwordx4 a[4:7], v38, s[8:11], s7 offen sc1\n"
"s_add_u32 s7, s7, 1024\n"
"s_and_b32 s7, s7, 0x7c00\n"
"buffer_load_dwordx4 a[8:11], v38, s[8:11], s7 offen sc1\n"
"s_add_u32 s7, s7, 1024\n"
"s_and_b32 s7, s7, 0x7c00\n"
"buffer_load_dwordx4 a[12:15], v38, s[8:11], s7 offen sc1\n"
"s_add_u32 s7, s7, 1024\n"
"s_and_b32 s7, s7, 0x7c00\n"
"buffer_load_dwordx4 a[16:19], v38, s[8:11], s7 offen sc1\n"
"s_add_u32 s7, s7, 1024\n"
"s_and_b32 s7, s7, 0x7c00\n"
"buffer_load_dwordx4 a[20:23], v38, s[8:11], s7 offen sc1\n"
"s_add_u32 s7, s7, 1024\n"
"s_and_b32 s7, s7, 0x7c00\n"
"buffer_load_dwordx4 a[24:27], v38, s[8:11], s7 offen sc1\n"
"s_add_u32 s7, s7, 1024\n"
"s_and_b32 s7, s7, 0x7c00\n"
"buffer_load_dwordx4 a[28:31], v38, s[8:11], s7 offen sc1\n"
"s_add_u32 s7, s7, 1024\n"
"s_and_b32 s7, s7, 0x7c00\n"
"ds_read_b64_tr_b16 v[44:45], v16 offset:0\n"
"ds_read_b64_tr_b16 v[46:47], v16 offset:1024\n"
"ds_read_b64_tr_b16 v[48:49], v18 offset:0\n"
"ds_read_b64_tr_b16 v[50:51], v18 offset:1024\n"
"ds_read_b64_tr_b16 v[54:55], v21 offset:0\n"
"ds_read_b64_tr_b16 v[56:57], v21 offset:1024\n"
"ds_read_b64_tr_b16 v[58:59], v23 offset:0\n"
"ds_read_b64_tr_b16 v[60:61], v23 offset:1024\n"
"v_lshl_add_u32 v41, v3, 11, v24\n"
"s_waitcnt vmcnt(7) lgkmcnt(6)\n"
"v_mfma_f32_32x32x16_bf16 v[90:105], v[44:47], a[0:3], 0\n"
"ds_read_b64_tr_b16 v[62:63], v16 offset:4096\n"
"ds_read_b64_tr_b16 v[64:65], v16 offset:5120\n"
"buffer_load_dwordx4 a[32:35], v38, s[8:11], s7 offen sc1\n"
"s_add_u32 s7, s7, 1024\n"
"s_and_b32 s7, s7, 0x7c00\n"
"s_waitcnt lgkmcnt(6)\n"
"v_mfma_f32_32x32x16_bf16 v[106:121], v[48:51], a[0:3], 0\n"
"ds_read_b64_tr_b16 v[66:67], v18 offset:4096\n"
"ds_read_b64_tr_b16 v[68:69], v18 offset:5120\n"
"s_waitcnt lgkmcnt(6)\n"
"v_mfma_f32_32x32x16_bf16 v[122:137], v[54:57], a[0:3], 0\n"
"ds_read_b64_tr_b16 v[70:71], v21 offset:4096\n"
"ds_read_b64_tr_b16 v[72:73], v21 offset:5120\n"
"buffer_load_dwordx4 a[36:39], v38, s[8:11], s7 offen sc1\n"
"s_add_u32 s7, s7, 1024\n"
"s_and_b32 s7, s7, 0x7c00\n"
"s_waitcnt lgkmcnt(6)\n"
"v_mfma_f32_32x32x16_bf16 v[138:153], v[58:61], a[0:3], 0\n"
"ds_read_b64_tr_b16 v[78:79], v23 offset:4096\n"
"ds_read_b64_tr_b16 v[80:81], v23 offset:5120\n"
"s_waitcnt vmcnt(8) lgkmcnt(6)\n"
"v_mfma_f32_32x32x16_bf16 v[90:105], v[62:65], a[4:7], v[90:105]\n"
"ds_read_b64_tr_b16 v[44:45], v16 offset:8192\n"
"ds_read_b64_tr_b16 v[46:47], v16 offset:9216\n"
"buffer_load_dwordx4 a[40:43], v38, s[8:11], s7 offen sc1\n"
"s_add_u32 s7, s7, 1024\n"
"s_and_b32 s7, s7, 0x7c00\n"
"s_waitcnt lgkmcnt(6)\n"
"v_mfma_f32_32x32x16_bf16 v[106:121], v[66:69], a[4:7], v[106:121]\n"
"ds_read_b64_tr_b16 v[48:49], v18 offset:8192\n"
"ds_read_b64_tr_b16 v[50:51], v18 offset:9216\n"
"s_waitcnt lgkmcnt(6)\n"
"v_mfma_f32_32x32x16_bf16 v[122:137], v[70:73], a[4:7], v[122:137]\n"
"ds_read_b64_tr_b16 v[54:55], v21 offset:8192\n"
"ds_read_b64_tr_b16 v[56:57], v21 offset:9216\n"
"buffer_load_dwordx4 a[44:47], v38, s[8:11], s7 offen sc1\n"
"s_add_u32 s7, s7, 1024\n"
"s_and_b32 s7, s7, 0x7c00\n"
"s_waitcnt lgkmcnt(6)\n"
"v_mfma_f32_32x32x16_bf16 v[138:153], v[78:81], a[4:7], v[138:153]\n"
"ds_read_b64_tr_b16 v[58:59], v23 offset:8192\n"
"ds_read_b64_tr_b16 v[60:61], v23 offset:9216\n"
"s_waitcnt vmcnt(9) lgkmcnt(6)\n"
"v_mfma_f32_32x32x16_bf16 v[90:105], v[44:47], a[8:11], v[90:105]\n"
"ds_read_b64_tr_b16 v[62:63], v16 offset:12288\n"
"ds_read_b64_tr_b16 v[64:65], v16 offset:13312\n"
"buffer_load_dwordx4 a[48:51], v38, s[8:11], s7 offen sc1\n"
"s_add_u32 s7, s7, 1024\n"
"s_and_b32 s7, s7, 0x7c00\n"
"s_waitcnt lgkmcnt(6)\n"
"v_mfma_f32_32x32x16_bf16 v[106:121], v[48:51], a[8:11], v[106:121]\n"
"ds_read_b64_tr_b16 v[66:67], v18 offset:12288\n"
"ds_read_b64_tr_b16 v[68:69], v18 offset:13312\n"
"s_waitcnt lgkmcnt(6)\n"
"v_mfma_f32_32x32x16_bf16 v[122:137], v[54:57], a[8:11], v[122:137]\n"
"ds_read_b64_tr_b16 v[70:71], v21 offset:12288\n"
"ds_read_b64_tr_b16 v[72:73], v21 offset:13312\n"
"buffer_load_dwordx4 a[52:55], v38, s[8:11], s7 offen sc1\n"
"s_add_u32 s7, s7, 1024\n"
"s_and_b32 s7, s7, 0x7c00\n"
"s_waitcnt lgkmcnt(6)\n"
"v_mfma_f32_32x32x16_bf16 v[138:153], v[58:61], a[8:11], v[138:153]\n"
"ds_read_b64_tr_b16 v[78:79], v23 offset:12288\n"
"ds_read_b64_tr_b16 v[80:81], v23 offset:13312\n"
"s_waitcnt vmcnt(10) lgkmcnt(6)\n"
"v_mfma_f32_32x32x16_bf16 v[90:105], v[62:65], a[12:15], v[90:105]\n"
"ds_read_b64_tr_b16 v[44:45], v16 offset:16384\n"
"ds_read_b64_tr_b16 v[46:47], v16 offset:17408\n"
"buffer_load_dwordx4 a[56:59], v38, s[8:11], s7 offen sc1\n"
"s_add_u32 s7, s7, 1024\n"
"s_and_b32 s7, s7, 0x7c00\n"
"s_waitcnt lgkmcnt(6)\n"
"v_mfma_f32_32x32x16_bf16 v[106:121], v[66:69], a[12:15], v[106:121]\n"
"ds_read_b64_tr_b16 v[48:49], v18 offset:16384\n"
"ds_read_b64_tr_b16 v[50:51], v18 offset:17408\n"
"s_waitcnt lgkmcnt(6)\n"
"v_mfma_f32_32x32x16_bf16 v[122:137], v[70:73], a[12:15], v[122:137]\n"
"ds_read_b64_tr_b16 v[54:55], v21 offset:16384\n"
"ds_read_b64_tr_b16 v[56:57], v21 offset:17408\n"
"buffer_load_dwordx4 a[60:63], v38, s[8:11], s7 offen sc1\n"
"s_add_u32 s7, s7, 1024\n"
"s_and_b32 s7, s7, 0x7c00\n"
"s_waitcnt lgkmcnt(6)\n"
"v_mfma_f32_32x32x16_bf16 v[138:153], v[78:81], a[12:15], v[138:153]\n"
"ds_read_b64_tr_b16 v[58:59], v23 offset:16384\n"
"ds_read_b64_tr_b16 v[60:61], v23 offset:17408\n"
"s_waitcnt vmcnt(11) lgkmcnt(6)\n"
"v_mfma_f32_32x32x16_bf16 v[90:105], v[44:47], a[16:19], v[90:105]\n"
"ds_read_b64_tr_b16 v[62:63], v16 offset:20480\n"
"ds_read_b64_tr_b16 v[64:65], v16 offset:21504\n"
"buffer_load_dwordx4 a[64:67], v38, s[8:11], s7 offen sc1\n"
"s_add_u32 s7, s7, 1024\n"
"s_and_b32 s7, s7, 0x7c00\n"
"s_waitcnt lgkmcnt(6)\n"
"v_mfma_f32_32x32x16_bf16 v[106:121], v[48:51], a[16:19], v[106:121]\n"
"ds_read_b64_tr_b16 v[66:67], v18 offset:20480\n"
"ds_read_b64_tr_b16 v[68:69], v18 offset:21504\n"
"s_waitcnt lgkmcnt(6)\n"
"v_mfma_f32_32x32x16_bf16 v[122:137], v[54:57], a[16:19], v[122:137]\n"
"ds_read_b64_tr_b16 v[70:71], v21 offset:20480\n"
"ds_read_b64_tr_b16 v[72:73], v21 offset:21504\n"
"buffer_load_dwordx4 a[68:71], v38, s[8:11], s7 offen sc1\n"
"s_add_u32 s7, s7, 1024\n"
"s_and_b32 s7, s7, 0x7c00\n"
"s_waitcnt lgkmcnt(6)\n"
"v_mfma_f32_32x32x16_bf16 v[138:153], v[58:61], a[16:19], v[138:153]\n"
"ds_read_b64_tr_b16 v[78:79], v23 offset:20480\n"
"ds_read_b64_tr_b16 v[80:81], v23 offset:21504\n"
"s_waitcnt vmcnt(12) lgkmcnt(6)\n"
"v_mfma_f32_32x32x16_bf16 v[90:105], v[62:65], a[20:23], v[90:105]\n"
"ds_read_b64_tr_b16 v[44:45], v16 offset:24576\n"
"ds_read_b64_tr_b16 v[46:47], v16 offset:25600\n"
"buffer_load_dwordx4 a[72:75], v38, s[8:11], s7 offen sc1\n"
"s_add_u32 s7, s7, 1024\n"
"s_and_b32 s7, s7, 0x7c00\n"
"s_waitcnt lgkmcnt(6)\n"
"v_mfma_f32_32x32x16_bf16 v[106:121], v[66:69], a[20:23], v[106:121]\n"
"ds_read_b64_tr_b16 v[48:49], v18 offset:24576\n"
"ds_read_b64_tr_b16 v[50:51], v18 offset:25600\n"
"s_waitcnt lgkmcnt(6)\n"
"v_mfma_f32_32x32x16_bf16 v[122:137], v[70:73], a[20:23], v[122:137]\n"
"ds_read_b64_tr_b16 v[54:55], v21 offset:24576\n"
"ds_read_b64_tr_b16 v[56:57], v21 offset:25600\n"
"buffer_load_dwordx4 a[76:79], v38, s[8:11], s7 offen sc1\n"
"s_add_u32 s7, s7, 1024\n"
"s_and_b32 s7, s7, 0x7c00\n"
"s_waitcnt lgkmcnt(6)\n"
"v_mfma_f32_32x32x16_bf16 v[138:153], v[78:81], a[20:23], v[138:153]\n"
"ds_read_b64_tr_b16 v[58:59], v23 offset:24576\n"
"ds_read_b64_tr_b16 v[60:61], v23 offset:25600\n"
"s_waitcnt vmcnt(13) lgkmcnt(6)\n"
"v_mfma_f32_32x32x16_bf16 v[90:105], v[44:47], a[24:27], v[90:105]\n"
"ds_read_b64_tr_b16 v[62:63], v16 offset:28672\n"
"ds_read_b64_tr_b16 v[64:65], v16 offset:29696\n"
"buffer_load_dwordx4 a[80:83], v38, s[8:11], s7 offen sc1\n"
"s_add_u32 s7, s7, 1024\n"
"s_and_b32 s7, s7, 0x7c00\n"
"s_waitcnt lgkmcnt(6)\n"
"v_mfma_f32_32x32x16_bf16 v[106:121], v[48:51], a[24:27], v[106:121]\n"
"ds_read_b64_tr_b16 v[66:67], v18 offset:28672\n"
"ds_read_b64_tr_b16 v[68:69], v18 offset:29696\n"
"s_waitcnt lgkmcnt(6)\n"
"v_mfma_f32_32x32x16_bf16 v[122:137], v[54:57], a[24:27], v[122:137]\n"
"ds_read_b64_tr_b16 v[70:71], v21 offset:28672\n"
"ds_read_b64_tr_b16 v[72:73], v21 offset:29696\n"
"buffer_load_dwordx4 a[84:87], v38, s[8:11], s7 offen sc1\n"
"s_add_u32 s7, s7, 1024\n"
"s_and_b32 s7, s7, 0x7c00\n"
"s_waitcnt lgkmcnt(6)\n"
"v_mfma_f32_32x32x16_bf16 v[138:153], v[58:61], a[24:27], v[138:153]\n"
"ds_read_b64_tr_b16 v[78:79], v23 offset:28672\n"
"ds_read_b64_tr_b16 v[80:81], v23 offset:29696\n"
"s_waitcnt vmcnt(14) lgkmcnt(6)\n"
"v_mfma_f32_32x32x16_bf16 v[90:105], v[62:65], a[28:31], v[90:105]\n"
"ds_read_b64_tr_b16 v[44:45], v16 offset:32768\n"
"ds_read_b64_tr_b16 v[46:47], v16 offset:33792\n"
"buffer_load_dwordx4 a[88:91], v38, s[8:11], s7 offen sc1\n"
"s_add_u32 s7, s7, 1024\n"
"s_and_b32 s7, s7, 0x7c00\n"
"s_waitcnt lgkmcnt(6)\n"
"v_mfma_f32_32x32x16_bf16 v[106:121], v[66:69], a[28:31], v[106:121]\n"
"ds_read_b64_tr_b16 v[48:49], v18 offset:32768\n"
"ds_read_b64_tr_b16 v[50:51], v18 offset:33792\n"
"s_waitcnt lgkmcnt(6)\n"
"v_mfma_f32_32x32x16_bf16 v[122:137], v[70:73], a[28:31], v[122:137]\n"
"ds_read_b64_tr_b16 v[54:55], v21 offset:32768\n"
"ds_read_b64_tr_b16 v[56:57], v21 offset:33792\n"
"buffer_load_dwordx4 a[92:95], v38, s[8:11], s7 offen sc1\n"
"s_add_u32 s7, s7, 1024\n"
"s_and_b32 s7, s7, 0x7c00\n"
"s_waitcnt lgkmcnt(6)\n"
"v_mfma_f32_32x32x16_bf16 v[138:153], v[78:81], a[28:31], v[138:153]\n"
"ds_read_b64_tr_b16 v[58:59], v23 offset:32768\n"
"ds_read_b64_tr_b16 v[60:61], v23 offset:33792\n"
"s_waitcnt vmcnt(15) lgkmcnt(6)\n"
"v_mfma_f32_32x32x16_bf16 v[90:105], v[44:47], a[32:35], v[90:105]\n"
"ds_read_b64_tr_b16 v[62:63], v16 offset:36864\n"
"ds_read_b64_tr_b16 v[64:65], v16 offset:37888\n"
"buffer_load_dwordx4 a[96:99], v38, s[8:11], s7 offen sc1\n"
"s_add_u32 s7, s7, 1024\n"
"s_and_b32 s7, s7, 0x7c00\n"
"s_waitcnt lgkmcnt(6)\n"
"v_mfma_f32_32x32x16_bf16 v[106:121], v[48:51], a[32:35], v[106:121]\n"
"ds_read_b64_tr_b16 v[66:67], v18 offset:36864\n"
"ds_read_b64_tr_b16 v[68:69], v18 offset:37888\n"
"s_waitcnt lgkmcnt(6)\n"
"v_mfma_f32_32x32x16_bf16 v[122:137], v[54:57], a[32:35], v[122:137]\n"
"ds_read_b64_tr_b16 v[70:71], v21 offset:36864\n"
"ds_read_b64_tr_b16 v[72:73], v21 offset:37888\n"
"buffer_load_dwordx4 a[100:103], v38, s[8:11], s7 offen sc1\n"
"s_add_u32 s7, s7, 1024\n"
"s_and_b32 s7, s7, 0x7c00\n"
"s_waitcnt lgkmcnt(6)\n"
"v_mfma_f32_32x32x16_bf16 v[138:153], v[58:61], a[32:35], v[138:153]\n"
"ds_read_b64_tr_b16 v[78:79], v23 offset:36864\n"
"ds_read_b64_tr_b16 v[80:81], v23 offset:37888\n"
"s_waitcnt vmcnt(16) lgkmcnt(6)\n"
"v_mfma_f32_32x32x16_bf16 v[90:105], v[62:65], a[36:39], v[90:105]\n"
"ds_read_b64_tr_b16 v[44:45], v16 offset:40960\n"
"ds_read_b64_tr_b16 v[46:47], v16 offset:41984\n"
"buffer_load_dwordx4 a[104:107], v38, s[8:11], s7 offen sc1\n"
"s_add_u32 s7, s7, 1024\n"
"s_and_b32 s7, s7, 0x7c00\n"
"s_waitcnt lgkmcnt(6)\n"
"v_mfma_f32_32x32x16_bf16 v[106:121], v[66:69], a[36:39], v[106:121]\n"
"ds_read_b64_tr_b16 v[48:49], v18 offset:40960\n"
"ds_read_b64_tr_b16 v[50:51], v18 offset:41984\n"
"s_waitcnt lgkmcnt(6)\n"
"v_mfma_f32_32x32x16_bf16 v[122:137], v[70:73], a[36:39], v[122:137]\n"
"ds_read_b64_tr_b16 v[54:55], v21 offset:40960\n"
"ds_read_b64_tr_b16 v[56:57], v21 offset:41984\n"
"buffer_load_dwordx4 a[108:111], v38, s[8:11], s7 offen sc1\n"
"s_add_u32 s7, s7, 1024\n"
"s_and_b32 s7, s7, 0x7c00\n"
"s_waitcnt lgkmcnt(6)\n"
"v_mfma_f32_32x32x16_bf16 v[138:153], v[78:81], a[36:39], v[138:153]\n"
"ds_read_b64_tr_b16 v[58:59], v23 offset:40960\n"
"ds_read_b64_tr_b16 v[60:61], v23 offset:41984\n"
"s_waitcnt vmcnt(17) lgkmcnt(6)\n"
"v_mfma_f32_32x32x16_bf16 v[90:105], v[44:47], a[40:43], v[90:105]\n"
"ds_read_b64_tr_b16 v[62:63], v16 offset:45056\n"
"ds_read_b64_tr_b16 v[64:65], v16 offset:46080\n"
"buffer_load_dwordx4 a[112:115], v38, s[8:11], s7 offen sc1\n"
"s_add_u32 s7, s7, 1024\n"
"s_and_b32 s7, s7, 0x7c00\n"
"s_waitcnt lgkmcnt(6)\n"
"v_mfma_f32_32x32x16_bf16 v[106:121], v[48:51], a[40:43], v[106:121]\n"
"ds_read_b64_tr_b16 v[66:67], v18 offset:45056\n"
"ds_read_b64_tr_b16 v[68:69], v18 offset:46080\n"
"s_waitcnt lgkmcnt(6)\n"
"v_mfma_f32_32x32x16_bf16 v[122:137], v[54:57], a[40:43], v[122:137]\n"
"ds_read_b64_tr_b16 v[70:71], v21 offset:45056\n"
"ds_read_b64_tr_b16 v[72:73], v21 offset:46080\n"
"buffer_load_dwordx4 a[116:119], v38, s[8:11], s7 offen sc1\n"
"s_add_u32 s7, s7, 1024\n"
"s_and_b32 s7, s7, 0x7c00\n"
"s_waitcnt lgkmcnt(6)\n"
"v_mfma_f32_32x32x16_bf16 v[138:153], v[58:61], a[40:43], v[138:153]\n"
"ds_read_b64_tr_b16 v[78:79], v23 offset:45056\n"
"ds_read_b64_tr_b16 v[80:81], v23 offset:46080\n"
"s_waitcnt vmcnt(18) lgkmcnt(6)\n"
"v_mfma_f32_32x32x16_bf16 v[90:105], v[62:65], a[44:47], v[90:105]\n"
"ds_read_b64_tr_b16 v[44:45], v16 offset:49152\n"
"ds_read_b64_tr_b16 v[46:47], v16 offset:50176\n"
"buffer_load_dwordx4 a[120:123], v38, s[8:11], s7 offen sc1\n"
"s_add_u32 s7, s7, 1024\n"
"s_and_b32 s7, s7, 0x7c00\n"
"s_waitcnt lgkmcnt(6)\n"
"v_mfma_f32_32x32x16_bf16 v[106:121], v[66:69], a[44:47], v[106:121]\n"
"ds_read_b64_tr_b16 v[48:49], v18 offset:49152\n"
"ds_read_b64_tr_b16 v[50:51], v18 offset:50176\n"
"s_waitcnt lgkmcnt(6)\n"
"v_mfma_f32_32x32x16_bf16 v[122:137], v[70:73], a[44:47], v[122:137]\n"
"ds_read_b64_tr_b16 v[54:55], v21 offset:49152\n"
"ds_read_b64_tr_b16 v[56:57], v21 offset:50176\n"
"buffer_load_dwordx4 a[124:127], v38, s[8:11], s7 offen sc1\n"
"s_add_u32 s7, s7, 1024\n"
"s_and_b32 s7, s7, 0x7c00\n"
"s_waitcnt lgkmcnt(6)\n"
"v_mfma_f32_32x32x16_bf16 v[138:153], v[78:81], a[44:47], v[138:153]\n"
"ds_read_b64_tr_b16 v[58:59], v23 offset:49152\n"
"ds_read_b64_tr_b16 v[60:61], v23 offset:50176\n"
"s_waitcnt vmcnt(19) lgkmcnt(6)\n"
"v_mfma_f32_32x32x16_bf16 v[90:105], v[44:47], a[48:51], v[90:105]\n"
"ds_read_b64_tr_b16 v[62:63], v16 offset:53248\n"
"ds_read_b64_tr_b16 v[64:65], v16 offset:54272\n"
"buffer_load_dwordx2 v[154:155], v41, s[8:11], s18 offen offset:0 sc1\n"
"s_waitcnt lgkmcnt(6)\n"
"v_mfma_f32_32x32x16_bf16 v[106:121], v[48:51], a[48:51], v[106:121]\n"
"ds_read_b64_tr_b16 v[66:67], v18 offset:53248\n"
"ds_read_b64_tr_b16 v[68:69], v18 offset:54272\n"
"s_waitcnt lgkmcnt(6)\n"
"v_mfma_f32_32x32x16_bf16 v[122:137], v[54:57], a[48:51], v[122:137]\n"
"ds_read_b64_tr_b16 v[70:71], v21 offset:53248\n"
"ds_read_b64_tr_b16 v[72:73], v21 offset:54272\n"
"buffer_load_dwordx2 v[156:157], v41, s[8:11], s18 offen offset:256 sc1\n"
"s_waitcnt lgkmcnt(6)\n"
"v_mfma_f32_32x32x16_bf16 v[138:153], v[58:61], a[48:51], v[138:153]\n"
"ds_read_b64_tr_b16 v[78:79], v23 offset:53248\n"
"ds_read_b64_tr_b16 v[80:81], v23 offset:54272\n"
"s_waitcnt vmcnt(20) lgkmcnt(6)\n"
"v_mfma_f32_32x32x16_bf16 v[90:105], v[62:65], a[52:55], v[90:105]\n"
"ds_read_b64_tr_b16 v[44:45], v16 offset:57344\n"
"ds_read_b64_tr_b16 v[46:47], v16 offset:58368\n"
"buffer_load_dwordx2 v[158:159], v41, s[8:11], s18 offen offset:512 sc1\n"
"s_waitcnt lgkmcnt(6)\n"
"v_mfma_f32_32x32x16_bf16 v[106:121], v[66:69], a[52:55], v[106:121]\n"
"ds_read_b64_tr_b16 v[48:49], v18 offset:57344\n"
"ds_read_b64_tr_b16 v[50:51], v18 offset:58368\n"
"s_waitcnt lgkmcnt(6)\n"
"v_mfma_f32_32x32x16_bf16 v[122:137], v[70:73], a[52:55], v[122:137]\n"
"ds_read_b64_tr_b16 v[54:55], v21 offset:57344\n"
"ds_read_b64_tr_b16 v[56:57], v21 offset:58368\n"
"buffer_load_dwordx2 v[160:161], v41, s[8:11], s18 offen offset:768 sc1\n"
"s_waitcnt lgkmcnt(6)\n"
"v_mfma_f32_32x32x16_bf16 v[138:153], v[78:81], a[52:55], v[138:153]\n"
"ds_read_b64_tr_b16 v[58:59], v23 offset:57344\n"
"ds_read_b64_tr_b16 v[60:61], v23 offset:58368\n"
"s_waitcnt vmcnt(21) lgkmcnt(6)\n"
"v_mfma_f32_32x32x16_bf16 v[90:105], v[44:47], a[56:59], v[90:105]\n"
"ds_read_b64_tr_b16 v[62:63], v16 offset:61440\n"
"ds_read_b64_tr_b16 v[64:65], v16 offset:62464\n"
"buffer_load_dwordx2 v[162:163], v41, s[8:11], s18 offen offset:1024 sc1\n"
"s_waitcnt lgkmcnt(6)\n"
"v_mfma_f32_32x32x16_bf16 v[106:121], v[48:51], a[56:59], v[106:121]\n"
"ds_read_b64_tr_b16 v[66:67], v18 offset:61440\n"
"ds_read_b64_tr_b16 v[68:69], v18 offset:62464\n"
"s_waitcnt lgkmcnt(6)\n"
"v_mfma_f32_32x32x16_bf16 v[122:137], v[54:57], a[56:59], v[122:137]\n"
"ds_read_b64_tr_b16 v[70:71], v21 offset:61440\n"
"ds_read_b64_tr_b16 v[72:73], v21 offset:62464\n"
"buffer_load_dwordx2 v[164:165], v41, s[8:11], s18 offen offset:1280 sc1\n"
"s_waitcnt lgkmcnt(6)\n"
"v_mfma_f32_32x32x16_bf16 v[138:153], v[58:61], a[56:59], v[138:153]\n"
"ds_read_b64_tr_b16 v[78:79], v23 offset:61440\n"
"ds_read_b64_tr_b16 v[80:81], v23 offset:62464\n"
"s_waitcnt vmcnt(22) lgkmcnt(6)\n"
"v_mfma_f32_32x32x16_bf16 v[90:105], v[62:65], a[60:63], v[90:105]\n"
"ds_read_b64_tr_b16 v[44:45], v17 offset:0\n"
"ds_read_b64_tr_b16 v[46:47], v17 offset:1024\n"
"buffer_load_dwordx2 v[166:167], v41, s[8:11], s18 offen offset:1536 sc1\n"
"s_waitcnt lgkmcnt(6)\n"
"v_mfma_f32_32x32x16_bf16 v[106:121], v[66:69], a[60:63], v[106:121]\n"
"ds_read_b64_tr_b16 v[48:49], v20 offset:0\n"
"ds_read_b64_tr_b16 v[50:51], v20 offset:1024\n"
"s_waitcnt lgkmcnt(6)\n"
"v_mfma_f32_32x32x16_bf16 v[122:137], v[70:73], a[60:63], v[122:137]\n"
"ds_read_b64_tr_b16 v[54:55], v22 offset:0\n"
"ds_read_b64_tr_b16 v[56:57], v22 offset:1024\n"
"buffer_load_dwordx2 v[168:169], v41, s[8:11], s18 offen offset:1792 sc1\n"
"s_waitcnt lgkmcnt(6)\n"
"v_mfma_f32_32x32x16_bf16 v[138:153], v[78:81], a[60:63], v[138:153]\n"
"ds_read_b64_tr_b16 v[58:59], v26 offset:0\n"
"ds_read_b64_tr_b16 v[60:61], v26 offset:1024\n"
"s_waitcnt vmcnt(23) lgkmcnt(6)\n"
"v_mfma_f32_32x32x16_bf16 v[90:105], v[44:47], a[64:67], v[90:105]\n"
"ds_read_b64_tr_b16 v[62:63], v17 offset:4096\n"
"ds_read_b64_tr_b16 v[64:65], v17 offset:5120\n"
"s_waitcnt lgkmcnt(6)\n"
"v_mfma_f32_32x32x16_bf16 v[106:121], v[48:51], a[64:67], v[106:121]\n"
"ds_read_b64_tr_b16 v[66:67], v20 offset:4096\n"
"ds_read_b64_tr_b16 v[68:69], v20 offset:5120\n"
"s_waitcnt lgkmcnt(6)\n"
"v_mfma_f32_32x32x16_bf16 v[122:137], v[54:57], a[64:67], v[122:137]\n"
"ds_read_b64_tr_b16 v[70:71], v22 offset:4096\n"
"ds_read_b64_tr_b16 v[72:73], v22 offset:5120\n"
"s_waitcnt lgkmcnt(6)\n"
"v_mfma_f32_32x32x16_bf16 v[138:153], v[58:61], a[64:67], v[138:153]\n"
"ds_read_b64_tr_b16 v[78:79], v26 offset:4096\n"
"ds_read_b64_tr_b16 v[80:81], v26 offset:5120\n"
"s_waitcnt vmcnt(22) lgkmcnt(6)\n"
"v_mfma_f32_32x32x16_bf16 v[90:105], v[62:65], a[68:71], v[90:105]\n"
"ds_read_b64_tr_b16 v[44:45], v17 offset:8192\n"
"ds_read_b64_tr_b16 v[46:47], v17 offset:9216\n"
"s_waitcnt lgkmcnt(6)\n"
"v_mfma_f32_32x32x16_bf16 v[106:121], v[66:69], a[68:71], v[106:121]\n"
"ds_read_b64_tr_b16 v[48:49], v20 offset:8192\n"
"ds_read_b64_tr_b16 v[50:51], v20 offset:9216\n"
"s_waitcnt lgkmcnt(6)\n"
"v_mfma_f32_32x32x16_bf16 v[122:137], v[70:73], a[68:71], v[122:137]\n"
"ds_read_b64_tr_b16 v[54:55], v22 offset:8192\n"
"ds_read_b64_tr_b16 v[56:57], v22 offset:9216\n"
"s_waitcnt lgkmcnt(6)\n"
"v_mfma_f32_32x32x16_bf16 v[138:153], v[78:81], a[68:71], v[138:153]\n"
"ds_read_b64_tr_b16 v[58:59], v26 offset:8192\n"
"ds_read_b64_tr_b16 v[60:61], v26 offset:9216\n"
"s_waitcnt vmcnt(21) lgkmcnt(6)\n"
"v_mfma_f32_32x32x16_bf16 v[90:105], v[44:47], a[72:75], v[90:105]\n"
"s_waitcnt lgkmcnt(0)\n"
"s_barrier\n"
"ds_read_b64_tr_b16 v[62:63], v17 offset:12288\n"
"ds_read_b64_tr_b16 v[64:65], v17 offset:13312\n"
"s_add_u32 m0, s6, 0\n"
"s_nop 0\n"
"buffer_load_dwordx4 v13, s[52:55], s4 offen lds\n"
"v_mfma_f32_32x32x16_bf16 v[106:121], v[48:51], a[72:75], v[106:121]\n"
"ds_read_b64_tr_b16 v[66:67], v20 offset:12288\n"
"ds_read_b64_tr_b16 v[68:69], v20 offset:13312\n"
"v_mfma_f32_32x32x16_bf16 v[122:137], v[54:57], a[72:75], v[122:137]\n"
"ds_read_b64_tr_b16 v[70:71], v22 offset:12288\n"
"ds_read_b64_tr_b16 v[72:73], v22 offset:13312\n"
"v_mfma_f32_32x32x16_bf16 v[138:153], v[58:61], a[72:75], v[138:153]\n"
"ds_read_b64_tr_b16 v[78:79], v26 offset:12288\n"
"ds_read_b64_tr_b16 v[80:81], v26 offset:13312\n"
"s_waitcnt vmcnt(21) lgkmcnt(6)\n"
"v_mfma_f32_32x32x16_bf16 v[90:105], v[62:65], a[76:79], v[90:105]\n"
"ds_read_b64_tr_b16 v[44:45], v17 offset:16384\n"
"ds_read_b64_tr_b16 v[46:47], v17 offset:17408\n"
"buffer_load_dwordx4 v14, s[52:55], s4 offen offset:1024 lds\n"
"s_waitcnt lgkmcnt(6)\n"
"v_mfma_f32_32x32x16_bf16 v[106:121], v[66:69], a[76:79], v[106:121]\n"
"ds_read_b64_tr_b16 v[48:49], v20 offset:16384\n"
"ds_read_b64_tr_b16 v[50:51], v20 offset:17408\n"
"s_waitcnt lgkmcnt(6)\n"
"v_mfma_f32_32x32x16_bf16 v[122:137], v[70:73], a[76:79], v[122:137]\n"
"ds_read_b64_tr_b16 v[54:55], v22 offset:16384\n"
"ds_read_b64_tr_b16 v[56:57], v22 offset:17408\n"
"s_waitcnt lgkmcnt(6)\n"
"v_mfma_f32_32x32x16_bf16 v[138:153], v[78:81], a[76:79], v[138:153]\n"
"ds_read_b64_tr_b16 v[58:59], v26 offset:16384\n"
"ds_read_b64_tr_b16 v[60:61], v26 offset:17408\n"
"s_waitcnt vmcnt(21) lgkmcnt(6)\n"
"v_mfma_f32_32x32x16_bf16 v[90:105], v[44:47], a[80:83], v[90:105]\n"
"ds_read_b64_tr_b16 v[62:63], v17 offset:20480\n"
"ds_read_b64_tr_b16 v[64:65], v17 offset:21504\n"
"buffer_load_dwordx4 v15, s[52:55], s4 offen offset:2048 lds\n"
"s_add_u32 s4, s4, 229376\n"
"s_cmp_ge_u32 s4, 1835008\n"
"s_cselect_b32 s43, 1835008, 0\n"
"s_sub_u32 s4, s4, s43\n"
"s_waitcnt lgkmcnt(6)\n"
"v_mfma_f32_32x32x16_bf16 v[106:121], v[48:51], a[80:83], v[106:121]\n"
"ds_read_b64_tr_b16 v[66:67], v20 offset:20480\n"
"ds_read_b64_tr_b16 v[68:69], v20 offset:21504\n"
"s_waitcnt lgkmcnt(6)\n"
"v_mfma_f32_32x32x16_bf16 v[122:137], v[54:57], a[80:83], v[122:137]\n"
"ds_read_b64_tr_b16 v[70:71], v22 offset:20480\n"
"ds_read_b64_tr_b16 v[72:73], v22 offset:21504\n"
"s_waitcnt lgkmcnt(6)\n"
"v_mfma_f32_32x32x16_bf16 v[138:153], v[58:61], a[80:83], v[138:153]\n"
"ds_read_b64_tr_b16 v[78:79], v26 offset:20480\n"
"ds_read_b64_tr_b16 v[80:81], v26 offset:21504\n"
"s_waitcnt vmcnt(21) lgkmcnt(6)\n"
"v_mfma_f32_32x32x16_bf16 v[90:105], v[62:65], a[84:87], v[90:105]\n"
"ds_read_b64_tr_b16 v[44:45], v17 offset:24576\n"
"ds_read_b64_tr_b16 v[46:47], v17 offset:25600\n"
"s_add_u32 m0, s6, 6144\n"
"s_nop 0\n"
"buffer_load_dwordx4 v13, s[52:55], s4 offen lds\n"
"s_waitcnt lgkmcnt(6)\n"
"v_mfma_f32_32x32x16_bf16 v[106:121], v[66:69], a[84:87], v[106:121]\n"
"ds_read_b64_tr_b16 v[48:49], v20 offset:24576\n"
"ds_read_b64_tr_b16 v[50:51], v20 offset:25600\n"
"s_waitcnt lgkmcnt(6)\n"
"v_mfma_f32_32x32x16_bf16 v[122:137], v[70:73], a[84:87], v[122:137]\n"
"ds_read_b64_tr_b16 v[54:55], v22 offset:24576\n"
"ds_read_b64_tr_b16 v[56:57], v22 offset:25600\n"
"s_waitcnt lgkmcnt(6)\n"
"v_mfma_f32_32x32x16_bf16 v[138:153], v[78:81], a[84:87], v[138:153]\n"
"ds_read_b64_tr_b16 v[58:59], v26 offset:24576\n"
"ds_read_b64_tr_b16 v[60:61], v26 offset:25600\n"
"s_waitcnt vmcnt(21) lgkmcnt(6)\n"
"v_mfma_f32_32x32x16_bf16 v[90:105], v[44:47], a[88:91], v[90:105]\n"
"ds_read_b64_tr_b16 v[62:63], v17 offset:28672\n"
"ds_read_b64_tr_b16 v[64:65], v17 offset:29696\n"
"buffer_load_dwordx4 v14, s[52:55], s4 offen offset:1024 lds\n"
"s_waitcnt lgkmcnt(6)\n"
"v_mfma_f32_32x32x16_bf16 v[106:121], v[48:51], a[88:91], v[106:121]\n"
"ds_read_b64_tr_b16 v[66:67], v20 offset:28672\n"
"ds_read_b64_tr_b16 v[68:69], v20 offset:29696\n"
"s_waitcnt lgkmcnt(6)\n"
"v_mfma_f32_32x32x16_bf16 v[122:137], v[54:57], a[88:91], v[122:137]\n"
"ds_read_b64_tr_b16 v[70:71], v22 offset:28672\n"
"ds_read_b64_tr_b16 v[72:73], v22 offset:29696\n"
"buffer_load_dwordx4 v15, s[52:55], s4 offen offset:2048 lds\n"
"s_add_u32 s4, s4, 229376\n"
"s_cmp_ge_u32 s4, 1835008\n"
"s_cselect_b32 s43, 1835008, 0\n"
"s_sub_u32 s4, s4, s43\n"
"s_waitcnt lgkmcnt(6)\n"
"v_mfma_f32_32x32x16_bf16 v[138:153], v[58:61], a[88:91], v[138:153]\n"
"ds_read_b64_tr_b16 v[78:79], v26 offset:28672\n"
"ds_read_b64_tr_b16 v[80:81], v26 offset:29696\n"
"s_waitcnt vmcnt(22) lgkmcnt(6)\n"
"v_mfma_f32_32x32x16_bf16 v[90:105], v[62:65], a[92:95], v[90:105]\n"
"ds_read_b64_tr_b16 v[44:45], v17 offset:32768\n"
"ds_read_b64_tr_b16 v[46:47], v17 offset:33792\n"
"s_add_u32 m0, s6, 12288\n"
"s_nop 0\n"
"buffer_load_dwordx4 v13, s[52:55], s4 offen lds\n"
"s_waitcnt lgkmcnt(6)\n"
"v_mfma_f32_32x32x16_bf16 v[106:121], v[66:69], a[92:95], v[106:121]\n"
"ds_read_b64_tr_b16 v[48:49], v20 offset:32768\n"
"ds_read_b64_tr_b16 v[50:51], v20 offset:33792\n"
"s_waitcnt lgkmcnt(6)\n"
"v_mfma_f32_32x32x16_bf16 v[122:137], v[70:73], a[92:95], v[122:137]\n"
"ds_read_b64_tr_b16 v[54:55], v22 offset:32768\n"
"ds_read_b64_tr_b16 v[56:57], v22 offset:33792\n"
"buffer_load_dwordx4 v14, s[52:55], s4 offen offset:1024 lds\n"
"s_waitcnt lgkmcnt(6)\n"
"v_mfma_f32_32x32x16_bf16 v[138:153], v[78:81], a[92:95], v[138:153]\n"
"ds_read_b64_tr_b16 v[58:59], v26 offset:32768\n"
"ds_read_b64_tr_b16 v[60:61], v26 offset:33792\n"
"s_waitcnt vmcnt(23) lgkmcnt(6)\n"
"v_mfma_f32_32x32x16_bf16 v[90:105], v[44:47], a[96:99], v[90:105]\n"
"ds_read_b64_tr_b16 v[62:63], v17 offset:36864\n"
"ds_read_b64_tr_b16 v[64:65], v17 offset:37888\n"
"buffer_load_dwordx4 v15, s[52:55], s4 offen offset:2048 lds\n"
"s_add_u32 s4, s4, 229376\n"
"s_cmp_ge_u32 s4, 1835008\n"
"s_cselect_b32 s43, 1835008, 0\n"
"s_sub_u32 s4, s4, s43\n"
"s_waitcnt lgkmcnt(6)\n"
"v_mfma_f32_32x32x16_bf16 v[106:121], v[48:51], a[96:99], v[106:121]\n"
"ds_read_b64_tr_b16 v[66:67], v20 offset:36864\n"
"ds_read_b64_tr_b16 v[68:69], v20 offset:37888\n"
"s_waitcnt lgkmcnt(6)\n"
"v_mfma_f32_32x32x16_bf16 v[122:137], v[54:57], a[96:99], v[122:137]\n"
"ds_read_b64_tr_b16 v[70:71], v22 offset:36864\n"
"ds_read_b64_tr_b16 v[72:73], v22 offset:37888\n"
"s_add_u32 m0, s6, 18432\n"
"s_nop 0\n"
"buffer_load_dwordx4 v13, s[52:55], s4 offen lds\n"
"s_waitcnt lgkmcnt(6)\n"
"v_mfma_f32_32x32x16_bf16 v[138:153], v[58:61], a[96:99], v[138:153]\n"
"ds_read_b64_tr_b16 v[78:79], v26 offset:36864\n"
"ds_read_b64_tr_b16 v[80:81], v26 offset:37888\n"
"s_waitcnt vmcnt(24) lgkmcnt(6)\n"
"v_mfma_f32_32x32x16_bf16 v[90:105], v[62:65], a[100:103], v[90:105]\n"
"ds_read_b64_tr_b16 v[44:45], v17 offset:40960\n"
"ds_read_b64_tr_b16 v[46:47], v17 offset:41984\n"
"buffer_load_dwordx4 v14, s[52:55], s4 offen offset:1024 lds\n"
"s_waitcnt lgkmcnt(6)\n"
"v_mfma_f32_32x32x16_bf16 v[106:121], v[66:69], a[100:103], v[106:121]\n"
"ds_read_b64_tr_b16 v[48:49], v20 offset:40960\n"
"ds_read_b64_tr_b16 v[50:51], v20 offset:41984\n"
"s_waitcnt lgkmcnt(6)\n"
"v_mfma_f32_32x32x16_bf16 v[122:137], v[70:73], a[100:103], v[122:137]\n"
"ds_read_b64_tr_b16 v[54:55], v22 offset:40960\n"
"ds_read_b64_tr_b16 v[56:57], v22 offset:41984\n"
"buffer_load_dwordx4 v15, s[52:55], s4 offen offset:2048 lds\n"
"s_add_u32 s4, s4, 229376\n"
"s_cmp_ge_u32 s4, 1835008\n"
"s_cselect_b32 s43, 1835008, 0\n"
"s_sub_u32 s4, s4, s43\n"
"s_waitcnt lgkmcnt(6)\n"
"v_mfma_f32_32x32x16_bf16 v[138:153], v[78:81], a[100:103], v[138:153]\n"
"ds_read_b64_tr_b16 v[58:59], v26 offset:40960\n"
"ds_read_b64_tr_b16 v[60:61], v26 offset:41984\n"
"s_waitcnt vmcnt(25) lgkmcnt(6)\n"
"v_mfma_f32_32x32x16_bf16 v[90:105], v[44:47], a[104:107], v[90:105]\n"
"ds_read_b64_tr_b16 v[62:63], v17 offset:45056\n"
"ds_read_b64_tr_b16 v[64:65], v17 offset:46080\n"
"s_add_u32 m0, s6, 24576\n"
"s_nop 0\n"
"buffer_load_dwordx4 v13, s[52:55], s4 offen lds\n"
"s_waitcnt lgkmcnt(6)\n"
"v_mfma_f32_32x32x16_bf16 v[106:121], v[48:51], a[104:107], v[106:121]\n"
"ds_read_b64_tr_b16 v[66:67], v20 offset:45056\n"
"ds_read_b64_tr_b16 v[68:69], v20 offset:46080\n"
"s_waitcnt lgkmcnt(6)\n"
"v_mfma_f32_32x32x16_bf16 v[122:137], v[54:57], a[104:107], v[122:137]\n"
"ds_read_b64_tr_b16 v[70:71], v22 offset:45056\n"
"ds_read_b64_tr_b16 v[72:73], v22 offset:46080\n"
"buffer_load_dwordx4 v14, s[52:55], s4 offen offset:1024 lds\n"
"s_waitcnt lgkmcnt(6)\n"
"v_mfma_f32_32x32x16_bf16 v[138:153], v[58:61], a[104:107], v[138:153]\n"
"ds_read_b64_tr_b16 v[78:79], v26 offset:45056\n"
"ds_read_b64_tr_b16 v[80:81], v26 offset:46080\n"
"s_waitcnt vmcnt(26) lgkmcnt(6)\n"
"v_mfma_f32_32x32x16_bf16 v[90:105], v[62:65], a[108:111], v[90:105]\n"
"ds_read_b64_tr_b16 v[44:45], v17 offset:49152\n"
"ds_read_b64_tr_b16 v[46:47], v17 offset:50176\n"
"buffer_load_dwordx4 v15, s[52:55], s4 offen offset:2048 lds\n"
"s_add_u32 s4, s4, 229376\n"
"s_cmp_ge_u32 s4, 1835008\n"
"s_cselect_b32 s43, 1835008, 0\n"
"s_sub_u32 s4, s4, s43\n"
"s_waitcnt lgkmcnt(6)\n"
"v_mfma_f32_32x32x16_bf16 v[106:121], v[66:69], a[108:111], v[106:121]\n"
"ds_read_b64_tr_b16 v[48:49], v20 offset:49152\n"
"ds_read_b64_tr_b16 v[50:51], v20 offset:50176\n"
"s_waitcnt lgkmcnt(6)\n"
"v_mfma_f32_32x32x16_bf16 v[122:137], v[70:73], a[108:111], v[122:137]\n"
"ds_read_b64_tr_b16 v[54:55], v22 offset:49152\n"
"ds_read_b64_tr_b16 v[56:57], v22 offset:50176\n"
"s_add_u32 m0, s6, 30720\n"
"s_nop 0\n"
"buffer_load_dwordx4 v13, s[52:55], s4 offen lds\n"
"s_waitcnt lgkmcnt(6)\n"
"v_mfma_f32_32x32x16_bf16 v[138:153], v[78:81], a[108:111], v[138:153]\n"
"ds_read_b64_tr_b16 v[58:59], v26 offset:49152\n"
"ds_read_b64_tr_b16 v[60:61], v26 offset:50176\n"
"s_waitcnt vmcnt(27) lgkmcnt(6)\n"
"v_mfma_f32_32x32x16_bf16 v[90:105], v[44:47], a[112:115], v[90:105]\n"
"ds_read_b64_tr_b16 v[62:63], v17 offset:53248\n"
"ds_read_b64_tr_b16 v[64:65], v17 offset:54272\n"
"buffer_load_dwordx4 v14, s[52:55], s4 offen offset:1024 lds\n"
"s_waitcnt lgkmcnt(6)\n"
"v_mfma_f32_32x32x16_bf16 v[106:121], v[48:51], a[112:115], v[106:121]\n"
"ds_read_b64_tr_b16 v[66:67], v20 offset:53248\n"
"ds_read_b64_tr_b16 v[68:69], v20 offset:54272\n"
"s_waitcnt lgkmcnt(6)\n"
"v_mfma_f32_32x32x16_bf16 v[122:137], v[54:57], a[112:115], v[122:137]\n"
"ds_read_b64_tr_b16 v[70:71], v22 offset:53248\n"
"ds_read_b64_tr_b16 v[72:73], v22 offset:54272\n"
"buffer_load_dwordx4 v15, s[52:55], s4 offen offset:2048 lds\n"
"s_add_u32 s4, s4, 229376\n"
"s_cmp_ge_u32 s4, 1835008\n"
"s_cselect_b32 s43, 1835008, 0\n"
"s_sub_u32 s4, s4, s43\n"
"s_waitcnt lgkmcnt(6)\n"
"v_mfma_f32_32x32x16_bf16 v[138:153], v[58:61], a[112:115], v[138:153]\n"
"ds_read_b64_tr_b16 v[78:79], v26 offset:53248\n"
"ds_read_b64_tr_b16 v[80:81], v26 offset:54272\n"
"s_waitcnt vmcnt(28) lgkmcnt(6)\n"
"v_mfma_f32_32x32x16_bf16 v[90:105], v[62:65], a[116:119], v[90:105]\n"
"ds_read_b64_tr_b16 v[44:45], v17 offset:57344\n"
"ds_read_b64_tr_b16 v[46:47], v17 offset:58368\n"
"s_waitcnt lgkmcnt(6)\n"
"v_mfma_f32_32x32x16_bf16 v[106:121], v[66:69], a[116:119], v[106:121]\n"
"ds_read_b64_tr_b16 v[48:49], v20 offset:57344\n"
"ds_read_b64_tr_b16 v[50:51], v20 offset:58368\n"
"s_waitcnt lgkmcnt(6)\n"
"v_mfma_f32_32x32x16_bf16 v[122:137], v[70:73], a[116:119], v[122:137]\n"
"ds_read_b64_tr_b16 v[54:55], v22 offset:57344\n"
"ds_read_b64_tr_b16 v[56:57], v22 offset:58368\n"
"s_waitcnt lgkmcnt(6)\n"
"v_mfma_f32_32x32x16_bf16 v[138:153], v[78:81], a[116:119], v[138:153]\n"
"ds_read_b64_tr_b16 v[58:59], v26 offset:57344\n"
"ds_read_b64_tr_b16 v[60:61], v26 offset:58368\n"
"s_waitcnt vmcnt(27) lgkmcnt(6)\n"
"v_mfma_f32_32x32x16_bf16 v[90:105], v[44:47], a[120:123], v[90:105]\n"
"ds_read_b64_tr_b16 v[62:63], v17 offset:61440\n"
"ds_read_b64_tr_b16 v[64:65], v17 offset:62464\n"
"s_waitcnt lgkmcnt(6)\n"
"v_mfma_f32_32x32x16_bf16 v[106:121], v[48:51], a[120:123], v[106:121]\n"
"ds_read_b64_tr_b16 v[66:67], v20 offset:61440\n"
"ds_read_b64_tr_b16 v[68:69], v20 offset:62464\n"
"s_waitcnt lgkmcnt(6)\n"
"v_mfma_f32_32x32x16_bf16 v[122:137], v[54:57], a[120:123], v[122:137]\n"
"ds_read_b64_tr_b16 v[70:71], v22 offset:61440\n"
"ds_read_b64_tr_b16 v[72:73], v22 offset:62464\n"
"s_waitcnt lgkmcnt(6)\n"
"v_mfma_f32_32x32x16_bf16 v[138:153], v[58:61], a[120:123], v[138:153]\n"
"ds_read_b64_tr_b16 v[78:79], v26 offset:61440\n"
"ds_read_b64_tr_b16 v[80:81], v26 offset:62464\n"
"s_waitcnt vmcnt(26) lgkmcnt(6)\n"
"v_mfma_f32_32x32x16_bf16 v[90:105], v[62:65], a[124:127], v[90:105]\n"
"s_waitcnt lgkmcnt(4)\n"
"v_mfma_f32_32x32x16_bf16 v[106:121], v[66:69], a[124:127], v[106:121]\n"
"s_waitcnt lgkmcnt(2)\n"
"v_mfma_f32_32x32x16_bf16 v[122:137], v[70:73], a[124:127], v[122:137]\n"
"s_waitcnt lgkmcnt(0)\n"
"v_mfma_f32_32x32x16_bf16 v[138:153], v[78:81], a[124:127], v[138:153]\n"
"s_waitcnt lgkmcnt(0)\n"
"s_barrier\n"
"ds_read_b32 v20, v37 offset:0\n"
"ds_read_b32 v21, v37 offset:256\n"
"ds_read_b32 v22, v37 offset:512\n"
"ds_read_b32 v23, v37 offset:768\n"
"ds_read_b64_tr_b16 v[44:45], v27 offset:0\n"
"ds_read_b64_tr_b16 v[46:47], v27 offset:768\n"
"ds_read_b64_tr_b16 v[48:49], v27 offset:32\n"
"ds_read_b64_tr_b16 v[50:51], v27 offset:800\n"
"ds_read_b64_tr_b16 v[54:55], v27 offset:64\n"
"ds_read_b64_tr_b16 v[56:57], v27 offset:832\n"
"ds_read_b64_tr_b16 v[58:59], v31 offset:0\n"
"ds_read_b64_tr_b16 v[60:61], v31 offset:768\n"
"s_waitcnt vmcnt(21)\n"
"v_pk_add_f32 v[154:155], v[154:155], v[162:163]\n"
"s_waitcnt vmcnt(20)\n"
"v_pk_add_f32 v[156:157], v[156:157], v[164:165]\n"
"s_waitcnt vmcnt(19)\n"
"v_pk_add_f32 v[158:159], v[158:159], v[166:167]\n"
"s_waitcnt vmcnt(18)\n"
"v_pk_add_f32 v[160:161], v[160:161], v[168:169]\n"
"v_pk_add_f32 v[154:155], v[154:155], v[158:159]\n"
"v_pk_add_f32 v[156:157], v[156:157], v[160:161]\n"
"v_pk_add_f32 v[154:155], v[154:155], v[156:157]\n"
"v_mov_b32 v5, v154\n"
"v_mov_b32 v6, v155\n"
"s_nop 0\n"
"v_permlane32_swap_b32 v154, v5\n"
"v_permlane32_swap_b32 v155, v6\n"
"v_add_f32 v154, v154, v5\n"
"v_add_f32 v155, v155, v6\n"
"v_mul_f32 v16, 0xbb000000, v154\n"
"v_mul_f32 v5, v16, v16\n"
"v_mov_b32 v6, 989855744\n"
"v_fma_f32 v5, v155, v6, -v5\n"
"v_max_f32 v5, 0, v5\n"
"v_add_f32 v5, 925353388, v5\n"
"v_rsq_f32 v5, v5\n"
"s_waitcnt lgkmcnt(11)\n"
"v_mfma_f32_32x32x2_f32 v[90:105], v20, v16, v[90:105]\n"
"s_waitcnt lgkmcnt(10)\n"
"v_mfma_f32_32x32x2_f32 v[106:121], v21, v16, v[106:121]\n"
"s_waitcnt lgkmcnt(9)\n"
"v_mfma_f32_32x32x2_f32 v[122:137], v22, v16, v[122:137]\n"
"s_waitcnt lgkmcnt(8)\n"
"v_mfma_f32_32x32x2_f32 v[138:153], v23, v16, v[138:153]\n"
"v_mul_f32 v17, 0xbfb8aa3b, v5\n"
"s_waitcnt lgkmcnt(6)\n"
"v_mfma_f32_32x32x16_bf16 v[154:169], v[44:47], a[0:3], 0\n"
"ds_read_b64_tr_b16 v[62:63], v27 offset:1536\n"
"ds_read_b64_tr_b16 v[64:65], v27 offset:2304\n"
"s_nop 6\n"
"v_mul_f32 v202, v17, v90\n"
"v_mul_f32 v203, v17, v91\n"
"v_mul_f32 v204, v17, v92\n"
"s_waitcnt lgkmcnt(6)\n"
"v_mfma_f32_32x32x16_bf16 v[170:185], v[48:51], a[0:3], 0\n"
"ds_read_b64_tr_b16 v[66:67], v27 offset:1568\n"
"ds_read_b64_tr_b16 v[68:69], v27 offset:2336\n"
"s_add_u32 m0, s66, 0\n"
"s_add_u32 s3, s65, 0\n"
"buffer_load_dwordx4 v39, s[56:59], s3 offen lds\n"
"v_mul_f32 v205, v17, v93\n"
"v_exp_f32 v202, v202\n"
"s_waitcnt lgkmcnt(6)\n"
"v_mfma_f32_32x32x16_bf16 v[186:201], v[54:57], a[0:3], 0\n"
"ds_read_b64_tr_b16 v[70:71], v27 offset:1600\n"
"ds_read_b64_tr_b16 v[72:73], v27 offset:2368\n"
"v_mul_f32 v90, v90, v98\n"
"v_exp_f32 v203, v203\n"
"v_mul_f32 v91, v91, v99\n"
"s_waitcnt lgkmcnt(7)\n"
"v_mfma_f32_4x4x4_16b_bf16 v[82:85], v[42:43], v[58:59], 0\n"
"s_waitcnt lgkmcnt(6)\n"
"v_mfma_f32_4x4x4_16b_bf16 v[86:89], v[42:43], v[60:61], 0\n"
"ds_read_b64_tr_b16 v[78:79], v31 offset:1536\n"
"ds_read_b64_tr_b16 v[80:81], v31 offset:2304\n"
"s_waitcnt lgkmcnt(6)\n"
"v_mfma_f32_32x32x16_bf16 v[154:169], v[62:65], a[4:7], v[154:169]\n"
"ds_read_b64_tr_b16 v[44:45], v27 offset:3072\n"
"ds_read_b64_tr_b16 v[46:47], v27 offset:3840\n"
"v_exp_f32 v204, v204\n"
"v_mul_f32 v92, v92, v100\n"
"v_exp_f32 v205, v205\n"
"s_waitcnt lgkmcnt(6)\n"
"v_mfma_f32_32x32x16_bf16 v[170:185], v[66:69], a[4:7], v[170:185]\n"
"ds_read_b64_tr_b16 v[48:49], v27 offset:3104\n"
"ds_read_b64_tr_b16 v[50:51], v27 offset:3872\n"
"buffer_load_dwordx4 v40, s[56:59], s3 offen offset:1024 lds\n"
"v_mul_f32 v93, v93, v101\n"
"v_add_f32 v202, 1.0, v202\n"
"s_waitcnt lgkmcnt(6)\n"
"v_mfma_f32_32x32x16_bf16 v[186:201], v[70:73], a[4:7], v[186:201]\n"
"ds_read_b64_tr_b16 v[54:55], v27 offset:3136\n"
"ds_read_b64_tr_b16 v[56:57], v27 offset:3904\n"
"v_add_f32 v203, 1.0, v203\n"
"v_add_f32 v204, 1.0, v204\n"
"v_add_f32 v205, 1.0, v205\n"
"s_waitcnt lgkmcnt(7)\n"
"v_mfma_f32_4x4x4_16b_bf16 v[82:85], v[42:43], v[78:79], v[82:85]\n"
"s_waitcnt lgkmcnt(6)\n"
"v_mfma_f32_4x4x4_16b_bf16 v[86:89], v[42:43], v[80:81], v[86:89]\n"
"ds_read_b64_tr_b16 v[58:59], v31 offset:3072\n"
"ds_read_b64_tr_b16 v[60:61], v31 offset:3840\n"
"s_waitcnt lgkmcnt(6)\n"
"v_mfma_f32_32x32x16_bf16 v[154:169], v[44:47], a[8:11], v[154:169]\n"
"ds_read_b64_tr_b16 v[62:63], v27 offset:4608\n"
"ds_read_b64_tr_b16 v[64:65], v27 offset:5376\n"
"v_rcp_f32 v202, v202\n"
"v_rcp_f32 v203, v203\n"
"v_rcp_f32 v204, v204\n"
"s_waitcnt lgkmcnt(6)\n"
"v_mfma_f32_32x32x16_bf16 v[170:185], v[48:51], a[8:11], v[170:185]\n"
"ds_read_b64_tr_b16 v[66:67], v27 offset:4640\n"
"ds_read_b64_tr_b16 v[68:69], v27 offset:5408\n"
"s_add_u32 m0, s66, 2048\n"
"s_add_u32 s3, s65, 16384\n"
"buffer_load_dwordx4 v39, s[56:59], s3 offen lds\n"
"v_rcp_f32 v205, v205\n"
"v_mul_f32 v90, v90, v202\n"
"s_waitcnt lgkmcnt(6)\n"
"v_mfma_f32_32x32x16_bf16 v[186:201], v[54:57], a[8:11], v[186:201]\n"
"ds_read_b64_tr_b16 v[70:71], v27 offset:4672\n"
"ds_read_b64_tr_b16 v[72:73], v27 offset:5440\n"
"v_mul_f32 v91, v91, v203\n"
"v_mul_f32 v92, v92, v204\n"
"v_mul_f32 v93, v93, v205\n"
"s_waitcnt lgkmcnt(7)\n"
"v_mfma_f32_4x4x4_16b_bf16 v[82:85], v[42:43], v[58:59], v[82:85]\n"
"s_waitcnt lgkmcnt(6)\n"
"v_mfma_f32_4x4x4_16b_bf16 v[86:89], v[42:43], v[60:61], v[86:89]\n"
"ds_read_b64_tr_b16 v[78:79], v31 offset:4608\n"
"ds_read_b64_tr_b16 v[80:81], v31 offset:5376\n"
"s_waitcnt lgkmcnt(6)\n"
"v_mfma_f32_32x32x16_bf16 v[154:169], v[62:65], a[12:15], v[154:169]\n"
"ds_read_b64_tr_b16 v[44:45], v27 offset:6144\n"
"ds_read_b64_tr_b16 v[46:47], v27 offset:6912\n"
"v_cvt_pk_bf16_f32 v206, v90, v91\n"
"v_cvt_pk_bf16_f32 v207, v92, v93\n"
"v_mul_f32 v202, v17, v94\n"
"s_waitcnt lgkmcnt(6)\n"
"v_mfma_f32_32x32x16_bf16 v[170:185], v[66:69], a[12:15], v[170:185]\n"
"ds_read_b64_tr_b16 v[48:49], v27 offset:6176\n"
"ds_read_b64_tr_b16 v[50:51], v27 offset:6944\n"
"buffer_load_dwordx4 v40, s[56:59], s3 offen offset:1024 lds\n"
"v_mul_f32 v203, v17, v95\n"
"v_mul_f32 v204, v17, v96\n"
"s_waitcnt lgkmcnt(6)\n"
"v_mfma_f32_32x32x16_bf16 v[186:201], v[70:73], a[12:15], v[186:201]\n"
"ds_read_b64_tr_b16 v[54:55], v27 offset:6208\n"
"ds_read_b64_tr_b16 v[56:57], v27 offset:6976\n"
"v_mul_f32 v205, v17, v97\n"
"v_exp_f32 v202, v202\n"
"v_mul_f32 v94, v94, v102\n"
"s_waitcnt lgkmcnt(7)\n"
"v_mfma_f32_4x4x4_16b_bf16 v[82:85], v[42:43], v[78:79], v[82:85]\n"
"s_waitcnt lgkmcnt(6)\n"
"v_mfma_f32_4x4x4_16b_bf16 v[86:89], v[42:43], v[80:81], v[86:89]\n"
"ds_read_b64_tr_b16 v[58:59], v31 offset:6144\n"
"ds_read_b64_tr_b16 v[60:61], v31 offset:6912\n"
"s_waitcnt lgkmcnt(6)\n"
"v_mfma_f32_32x32x16_bf16 v[154:169], v[44:47], a[16:19], v[154:169]\n"
"ds_read_b64_tr_b16 v[62:63], v27 offset:7680\n"
"ds_read_b64_tr_b16 v[64:65], v27 offset:8448\n"
"v_exp_f32 v203, v203\n"
"v_mul_f32 v95, v95, v103\n"
"v_exp_f32 v204, v204\n"
"s_waitcnt lgkmcnt(6)\n"
"v_mfma_f32_32x32x16_bf16 v[170:185], v[48:51], a[16:19], v[170:185]\n"
"ds_read_b64_tr_b16 v[66:67], v27 offset:7712\n"
"ds_read_b64_tr_b16 v[68:69], v27 offset:8480\n"
"s_add_u32 m0, s66, 4096\n"
"s_add_u32 s3, s65, 32768\n"
"buffer_load_dwordx4 v39, s[56:59], s3 offen lds\n"
"v_mul_f32 v96, v96, v104\n"
"v_exp_f32 v205, v205\n"
"s_waitcnt lgkmcnt(6)\n"
"v_mfma_f32_32x32x16_bf16 v[186:201], v[54:57], a[16:19], v[186:201]\n"
"ds_read_b64_tr_b16 v[70:71], v27 offset:7744\n"
"ds_read_b64_tr_b16 v[72:73], v27 offset:8512\n"
"v_mul_f32 v97, v97, v105\n"
"v_add_f32 v202, 1.0, v202\n"
"v_add_f32 v203, 1.0, v203\n"
"s_waitcnt lgkmcnt(7)\n"
"v_mfma_f32_4x4x4_16b_bf16 v[82:85], v[42:43], v[58:59], v[82:85]\n"
"s_waitcnt lgkmcnt(6)\n"
"v_mfma_f32_4x4x4_16b_bf16 v[86:89], v[42:43], v[60:61], v[86:89]\n"
"ds_read_b64_tr_b16 v[78:79], v31 offset:7680\n"
"ds_read_b64_tr_b16 v[80:81], v31 offset:8448\n"
"s_waitcnt lgkmcnt(6)\n"
"v_mfma_f32_32x32x16_bf16 v[154:169], v[62:65], a[20:23], v[154:169]\n"
"ds_read_b64_tr_b16 v[44:45], v27 offset:9216\n"
"ds_read_b64_tr_b16 v[46:47], v27 offset:9984\n"
"v_add_f32 v204, 1.0, v204\n"
"v_add_f32 v205, 1.0, v205\n"
"v_rcp_f32 v202, v202\n"
"s_waitcnt lgkmcnt(6)\n"
"v_mfma_f32_32x32x16_bf16 v[170:185], v[66:69], a[20:23], v[170:185]\n"
"ds_read_b64_tr_b16 v[48:49], v27 offset:9248\n"
"ds_read_b64_tr_b16 v[50:51], v27 offset:10016\n"
"buffer_load_dwordx4 v40, s[56:59], s3 offen offset:1024 lds\n"
"v_rcp_f32 v203, v203\n"
"v_rcp_f32 v204, v204\n"
"s_waitcnt lgkmcnt(6)\n"
"v_mfma_f32_32x32x16_bf16 v[186:201], v[70:73], a[20:23], v[186:201]\n"
"ds_read_b64_tr_b16 v[54:55], v27 offset:9280\n"
"ds_read_b64_tr_b16 v[56:57], v27 offset:10048\n"
"v_rcp_f32 v205, v205\n"
"v_mul_f32 v94, v94, v202\n"
"v_mul_f32 v95, v95, v203\n"
"s_waitcnt lgkmcnt(7)\n"
"v_mfma_f32_4x4x4_16b_bf16 v[82:85], v[42:43], v[78:79], v[82:85]\n"
"s_waitcnt lgkmcnt(6)\n"
"v_mfma_f32_4x4x4_16b_bf16 v[86:89], v[42:43], v[80:81], v[86:89]\n"
"ds_read_b64_tr_b16 v[58:59], v31 offset:9216\n"
"ds_read_b64_tr_b16 v[60:61], v31 offset:9984\n"
"s_waitcnt lgkmcnt(6)\n"
"v_mfma_f32_32x32x16_bf16 v[154:169], v[44:47], a[24:27], v[154:169]\n"
"ds_read_b64_tr_b16 v[62:63], v27 offset:10752\n"
"ds_read_b64_tr_b16 v[64:65], v27 offset:11520\n"
"v_mul_f32 v96, v96, v204\n"
"v_mul_f32 v97, v97, v205\n"
"v_cvt_pk_bf16_f32 v208, v94, v95\n"
"s_waitcnt lgkmcnt(6)\n"
"v_mfma_f32_32x32x16_bf16 v[170:185], v[48:51], a[24:27], v[170:185]\n"
"ds_read_b64_tr_b16 v[66:67], v27 offset:10784\n"
"ds_read_b64_tr_b16 v[68:69], v27 offset:11552\n"
"s_add_u32 m0, s66, 6144\n"
"s_add_u32 s3, s65, 49152\n"
"buffer_load_dwordx4 v39, s[56:59], s3 offen lds\n"
"v_cvt_pk_bf16_f32 v209, v96, v97\n"
"buffer_store_dwordx4 v[206:209], v4, s[8:11], s19 offen sc1\n"
"s_waitcnt lgkmcnt(6)\n"
"v_mfma_f32_32x32x16_bf16 v[186:201], v[54:57], a[24:27], v[186:201]\n"
"ds_read_b64_tr_b16 v[70:71], v27 offset:10816\n"
"ds_read_b64_tr_b16 v[72:73], v27 offset:11584\n"
"v_mul_f32 v202, v17, v106\n"
"v_mul_f32 v203, v17, v107\n"
"v_mul_f32 v204, v17, v108\n"
"s_waitcnt lgkmcnt(7)\n"
"v_mfma_f32_4x4x4_16b_bf16 v[82:85], v[42:43], v[58:59], v[82:85]\n"
"s_waitcnt lgkmcnt(6)\n"
"v_mfma_f32_4x4x4_16b_bf16 v[86:89], v[42:43], v[60:61], v[86:89]\n"
"ds_read_b64_tr_b16 v[78:79], v31 offset:10752\n"
"ds_read_b64_tr_b16 v[80:81], v31 offset:11520\n"
"s_waitcnt lgkmcnt(6)\n"
"v_mfma_f32_32x32x16_bf16 v[154:169], v[62:65], a[28:31], v[154:169]\n"
"s_waitcnt vmcnt(20)\n"
"s_waitcnt lgkmcnt(0)\n"
"s_barrier\n"
"ds_read_b64_tr_b16 v[44:45], v28 offset:0\n"
"ds_read_b64_tr_b16 v[46:47], v28 offset:768\n"
"v_mul_f32 v205, v17, v109\n"
"v_exp_f32 v202, v202\n"
"v_mul_f32 v106, v106, v114\n"
"v_mfma_f32_32x32x16_bf16 v[170:185], v[66:69], a[28:31], v[170:185]\n"
"ds_read_b64_tr_b16 v[48:49], v28 offset:32\n"
"ds_read_b64_tr_b16 v[50:51], v28 offset:800\n"
"buffer_load_dwordx4 v40, s[56:59], s3 offen offset:1024 lds\n"
"v_exp_f32 v203, v203\n"
"v_mul_f32 v107, v107, v115\n"
"v_mfma_f32_32x32x16_bf16 v[186:201], v[70:73], a[28:31], v[186:201]\n"
"ds_read_b64_tr_b16 v[54:55], v28 offset:64\n"
"ds_read_b64_tr_b16 v[56:57], v28 offset:832\n"
"v_exp_f32 v204, v204\n"
"v_mul_f32 v108, v108, v116\n"
"v_exp_f32 v205, v205\n"
"v_mfma_f32_4x4x4_16b_bf16 v[82:85], v[42:43], v[78:79], v[82:85]\n"
"v_mfma_f32_4x4x4_16b_bf16 v[86:89], v[42:43], v[80:81], v[86:89]\n"
"ds_read_b64_tr_b16 v[58:59], v32 offset:0\n"
"ds_read_b64_tr_b16 v[60:61], v32 offset:768\n"
"s_waitcnt lgkmcnt(6)\n"
"v_mfma_f32_32x32x16_bf16 v[154:169], v[44:47], a[32:35], v[154:169]\n"
"ds_read_b64_tr_b16 v[62:63], v28 offset:1536\n"
"ds_read_b64_tr_b16 v[64:65], v28 offset:2304\n"
"v_mul_f32 v109, v109, v117\n"
"v_add_f32 v202, 1.0, v202\n"
"v_add_f32 v203, 1.0, v203\n"
"s_waitcnt lgkmcnt(6)\n"
"v_mfma_f32_32x32x16_bf16 v[170:185], v[48:51], a[32:35], v[170:185]\n"
"ds_read_b64_tr_b16 v[66:67], v28 offset:1568\n"
"ds_read_b64_tr_b16 v[68:69], v28 offset:2336\n"
"s_add_u32 m0, s66, 8192\n"
"s_add_u32 s3, s65, 131072\n"
"buffer_load_dwordx4 v39, s[56:59], s3 offen lds\n"
"v_add_f32 v204, 1.0, v204\n"
"v_add_f32 v205, 1.0, v205\n"
"s_waitcnt lgkmcnt(6)\n"
"v_mfma_f32_32x32x16_bf16 v[186:201], v[54:57], a[32:35], v[186:201]\n"
"ds_read_b64_tr_b16 v[70:71], v28 offset:1600\n"
"ds_read_b64_tr_b16 v[72:73], v28 offset:2368\n"
"v_rcp_f32 v202, v202\n"
"v_rcp_f32 v203, v203\n"
"v_rcp_f32 v204, v204\n"
"s_waitcnt lgkmcnt(7)\n"
"v_mfma_f32_4x4x4_16b_bf16 v[82:85], v[42:43], v[58:59], v[82:85]\n"
"s_waitcnt lgkmcnt(6)\n"
"v_mfma_f32_4x4x4_16b_bf16 v[86:89], v[42:43], v[60:61], v[86:89]\n"
"ds_read_b64_tr_b16 v[78:79], v32 offset:1536\n"
"ds_read_b64_tr_b16 v[80:81], v32 offset:2304\n"
"s_waitcnt lgkmcnt(6)\n"
"v_mfma_f32_32x32x16_bf16 v[154:169], v[62:65], a[36:39], v[154:169]\n"
"ds_read_b64_tr_b16 v[44:45], v28 offset:3072\n"
"ds_read_b64_tr_b16 v[46:47], v28 offset:3840\n"
"v_rcp_f32 v205, v205\n"
"v_mul_f32 v106, v106, v202\n"
"v_mul_f32 v107, v107, v203\n"
"s_waitcnt lgkmcnt(6)\n"
"v_mfma_f32_32x32x16_bf16 v[170:185], v[66:69], a[36:39], v[170:185]\n"
"ds_read_b64_tr_b16 v[48:49], v28 offset:3104\n"
"ds_read_b64_tr_b16 v[50:51], v28 offset:3872\n"
"buffer_load_dwordx4 v40, s[56:59], s3 offen offset:1024 lds\n"
"v_mul_f32 v108, v108, v204\n"
"v_mul_f32 v109, v109, v205\n"
"s_waitcnt lgkmcnt(6)\n"
"v_mfma_f32_32x32x16_bf16 v[186:201], v[70:73], a[36:39], v[186:201]\n"
"ds_read_b64_tr_b16 v[54:55], v28 offset:3136\n"
"ds_read_b64_tr_b16 v[56:57], v28 offset:3904\n"
"v_cvt_pk_bf16_f32 v210, v106, v107\n"
"v_cvt_pk_bf16_f32 v211, v108, v109\n"
"v_mul_f32 v202, v17, v110\n"
"s_waitcnt lgkmcnt(7)\n"
"v_mfma_f32_4x4x4_16b_bf16 v[82:85], v[42:43], v[78:79], v[82:85]\n"
"s_waitcnt lgkmcnt(6)\n"
"v_mfma_f32_4x4x4_16b_bf16 v[86:89], v[42:43], v[80:81], v[86:89]\n"
"ds_read_b64_tr_b16 v[58:59], v32 offset:3072\n"
"ds_read_b64_tr_b16 v[60:61], v32 offset:3840\n"
"s_waitcnt lgkmcnt(6)\n"
"v_mfma_f32_32x32x16_bf16 v[154:169], v[44:47], a[40:43], v[154:169]\n"
"ds_read_b64_tr_b16 v[62:63], v28 offset:4608\n"
"ds_read_b64_tr_b16 v[64:65], v28 offset:5376\n"
"v_mul_f32 v203, v17, v111\n"
"v_mul_f32 v204, v17, v112\n"
"v_mul_f32 v205, v17, v113\n"
"s_waitcnt lgkmcnt(6)\n"
"v_mfma_f32_32x32x16_bf16 v[170:185], v[48:51], a[40:43], v[170:185]\n"
"ds_read_b64_tr_b16 v[66:67], v28 offset:4640\n"
"ds_read_b64_tr_b16 v[68:69], v28 offset:5408\n"
"s_add_u32 m0, s66, 10240\n"
"s_add_u32 s3, s65, 147456\n"
"buffer_load_dwordx4 v39, s[56:59], s3 offen lds\n"
"v_exp_f32 v202, v202\n"
"v_mul_f32 v110, v110, v118\n"
"s_waitcnt lgkmcnt(6)\n"
"v_mfma_f32_32x32x16_bf16 v[186:201], v[54:57], a[40:43], v[186:201]\n"
"ds_read_b64_tr_b16 v[70:71], v28 offset:4672\n"
"ds_read_b64_tr_b16 v[72:73], v28 offset:5440\n"
"v_exp_f32 v203, v203\n"
"v_mul_f32 v111, v111, v119\n"
"v_exp_f32 v204, v204\n"
"s_waitcnt lgkmcnt(7)\n"
"v_mfma_f32_4x4x4_16b_bf16 v[82:85], v[42:43], v[58:59], v[82:85]\n"
"s_waitcnt lgkmcnt(6)\n"
"v_mfma_f32_4x4x4_16b_bf16 v[86:89], v[42:43], v[60:61], v[86:89]\n"
"ds_read_b64_tr_b16 v[78:79], v32 offset:4608\n"
"ds_read_b64_tr_b16 v[80:81], v32 offset:5376\n"
"s_waitcnt lgkmcnt(6)\n"
"v_mfma_f32_32x32x16_bf16 v[154:169], v[62:65], a[44:47], v[154:169]\n"
"ds_read_b64_tr_b16 v[44:45], v28 offset:6144\n"
"ds_read_b64_tr_b16 v[46:47], v28 offset:6912\n"
"v_mul_f32 v112, v112, v120\n"
"v_exp_f32 v205, v205\n"
"v_mul_f32 v113, v113, v121\n"
"s_waitcnt lgkmcnt(6)\n"
"v_mfma_f32_32x32x16_bf16 v[170:185], v[66:69], a[44:47], v[170:185]\n"
"ds_read_b64_tr_b16 v[48:49], v28 offset:6176\n"
"ds_read_b64_tr_b16 v[50:51], v28 offset:6944\n"
"buffer_load_dwordx4 v40, s[56:59], s3 offen offset:1024 lds\n"
"v_add_f32 v202, 1.0, v202\n"
"v_add_f32 v203, 1.0, v203\n"
"s_waitcnt lgkmcnt(6)\n"
"v_mfma_f32_32x32x16_bf16 v[186:201], v[70:73], a[44:47], v[186:201]\n"
"ds_read_b64_tr_b16 v[54:55], v28 offset:6208\n"
"ds_read_b64_tr_b16 v[56:57], v28 offset:6976\n"
"v_add_f32 v204, 1.0, v204\n"
"v_add_f32 v205, 1.0, v205\n"
"v_rcp_f32 v202, v202\n"
"s_waitcnt lgkmcnt(7)\n"
"v_mfma_f32_4x4x4_16b_bf16 v[82:85], v[42:43], v[78:79], v[82:85]\n"
"s_waitcnt lgkmcnt(6)\n"
"v_mfma_f32_4x4x4_16b_bf16 v[86:89], v[42:43], v[80:81], v[86:89]\n"
"ds_read_b64_tr_b16 v[58:59], v32 offset:6144\n"
"ds_read_b64_tr_b16 v[60:61], v32 offset:6912\n"
"s_waitcnt lgkmcnt(6)\n"
"v_mfma_f32_32x32x16_bf16 v[154:169], v[44:47], a[48:51], v[154:169]\n"
"ds_read_b64_tr_b16 v[62:63], v28 offset:7680\n"
"ds_read_b64_tr_b16 v[64:65], v28 offset:8448\n"
"v_rcp_f32 v203, v203\n"
"v_rcp_f32 v204, v204\n"
"v_rcp_f32 v205, v205\n"
"s_waitcnt lgkmcnt(6)\n"
"v_mfma_f32_32x32x16_bf16 v[170:185], v[48:51], a[48:51], v[170:185]\n"
"ds_read_b64_tr_b16 v[66:67], v28 offset:7712\n"
"ds_read_b64_tr_b16 v[68:69], v28 offset:8480\n"
"s_add_u32 m0, s67, 0\n"
"s_add_u32 s3, s65, 163840\n"
"buffer_load_dwordx4 v39, s[56:59], s3 offen lds\n"
"v_mul_f32 v110, v110, v202\n"
"v_mul_f32 v111, v111, v203\n"
"s_waitcnt lgkmcnt(6)\n"
"v_mfma_f32_32x32x16_bf16 v[186:201], v[54:57], a[48:51], v[186:201]\n"
"ds_read_b64_tr_b16 v[70:71], v28 offset:7744\n"
"ds_read_b64_tr_b16 v[72:73], v28 offset:8512\n"
"v_mul_f32 v112, v112, v204\n"
"v_mul_f32 v113, v113, v205\n"
"v_cvt_pk_bf16_f32 v212, v110, v111\n"
"s_waitcnt lgkmcnt(7)\n"
"v_mfma_f32_4x4x4_16b_bf16 v[82:85], v[42:43], v[58:59], v[82:85]\n"
"s_waitcnt lgkmcnt(6)\n"
"v_mfma_f32_4x4x4_16b_bf16 v[86:89], v[42:43], v[60:61], v[86:89]\n"
"ds_read_b64_tr_b16 v[78:79], v32 offset:7680\n"
"ds_read_b64_tr_b16 v[80:81], v32 offset:8448\n"
"s_waitcnt lgkmcnt(6)\n"
"v_mfma_f32_32x32x16_bf16 v[154:169], v[62:65], a[52:55], v[154:169]\n"
"ds_read_b64_tr_b16 v[44:45], v28 offset:9216\n"
"ds_read_b64_tr_b16 v[46:47], v28 offset:9984\n"
"v_cvt_pk_bf16_f32 v213, v112, v113\n"
"buffer_store_dwordx4 v[210:213], v4, s[8:11], s19 offen offset:1024 sc1\n"
"v_mul_f32 v202, v17, v122\n"
"s_waitcnt lgkmcnt(6)\n"
"v_mfma_f32_32x32x16_bf16 v[170:185], v[66:69], a[52:55], v[170:185]\n"
"ds_read_b64_tr_b16 v[48:49], v28 offset:9248\n"
"ds_read_b64_tr_b16 v[50:51], v28 offset:10016\n"
"buffer_load_dwordx4 v40, s[56:59], s3 offen offset:1024 lds\n"
"v_mul_f32 v203, v17, v123\n"
"v_mul_f32 v204, v17, v124\n"
"s_waitcnt lgkmcnt(6)\n"
"v_mfma_f32_32x32x16_bf16 v[186:201], v[70:73], a[52:55], v[186:201]\n"
"ds_read_b64_tr_b16 v[54:55], v28 offset:9280\n"
"ds_read_b64_tr_b16 v[56:57], v28 offset:10048\n"
"v_mul_f32 v205, v17, v125\n"
"v_exp_f32 v202, v202\n"
"v_mul_f32 v122, v122, v130\n"
"s_waitcnt lgkmcnt(7)\n"
"v_mfma_f32_4x4x4_16b_bf16 v[82:85], v[42:43], v[78:79], v[82:85]\n"
"s_waitcnt lgkmcnt(6)\n"
"v_mfma_f32_4x4x4_16b_bf16 v[86:89], v[42:43], v[80:81], v[86:89]\n"
"ds_read_b64_tr_b16 v[58:59], v32 offset:9216\n"
"ds_read_b64_tr_b16 v[60:61], v32 offset:9984\n"
"s_waitcnt lgkmcnt(6)\n"
"v_mfma_f32_32x32x16_bf16 v[154:169], v[44:47], a[56:59], v[154:169]\n"
"ds_read_b64_tr_b16 v[62:63], v28 offset:10752\n"
"ds_read_b64_tr_b16 v[64:65], v28 offset:11520\n"
"v_exp_f32 v203, v203\n"
"v_mul_f32 v123, v123, v131\n"
"v_exp_f32 v204, v204\n"
"s_waitcnt lgkmcnt(6)\n"
"v_mfma_f32_32x32x16_bf16 v[170:185], v[48:51], a[56:59], v[170:185]\n"
"ds_read_b64_tr_b16 v[66:67], v28 offset:10784\n"
"ds_read_b64_tr_b16 v[68:69], v28 offset:11552\n"
"s_add_u32 m0, s67, 2048\n"
"s_add_u32 s3, s65, 180224\n"
"buffer_load_dwordx4 v39, s[56:59], s3 offen lds\n"
"v_mul_f32 v124, v124, v132\n"
"v_exp_f32 v205, v205\n"
"s_waitcnt lgkmcnt(6)\n"
"v_mfma_f32_32x32x16_bf16 v[186:201], v[54:57], a[56:59], v[186:201]\n"
"ds_read_b64_tr_b16 v[70:71], v28 offset:10816\n"
"ds_read_b64_tr_b16 v[72:73], v28 offset:11584\n"
"v_mul_f32 v125, v125, v133\n"
"v_add_f32 v202, 1.0, v202\n"
"v_add_f32 v203, 1.0, v203\n"
"s_waitcnt lgkmcnt(7)\n"
"v_mfma_f32_4x4x4_16b_bf16 v[82:85], v[42:43], v[58:59], v[82:85]\n"
"s_waitcnt lgkmcnt(6)\n"
"v_mfma_f32_4x4x4_16b_bf16 v[86:89], v[42:43], v[60:61], v[86:89]\n"
"ds_read_b64_tr_b16 v[78:79], v32 offset:10752\n"
"ds_read_b64_tr_b16 v[80:81], v32 offset:11520\n"
"s_waitcnt lgkmcnt(6)\n"
"v_mfma_f32_32x32x16_bf16 v[154:169], v[62:65], a[60:63], v[154:169]\n"
"s_waitcnt vmcnt(23)\n"
"s_waitcnt lgkmcnt(0)\n"
"s_barrier\n"
"ds_read_b64_tr_b16 v[44:45], v28 offset:12288\n"
"ds_read_b64_tr_b16 v[46:47], v28 offset:13056\n"
"v_add_f32 v204, 1.0, v204\n"
"v_add_f32 v205, 1.0, v205\n"
"v_rcp_f32 v202, v202\n"
"v_mfma_f32_32x32x16_bf16 v[170:185], v[66:69], a[60:63], v[170:185]\n"
"ds_read_b64_tr_b16 v[48:49], v28 offset:12320\n"
"ds_read_b64_tr_b16 v[50:51], v28 offset:13088\n"
"buffer_load_dwordx4 v40, s[56:59], s3 offen offset:1024 lds\n"
"v_rcp_f32 v203, v203\n"
"v_rcp_f32 v204, v204\n"
"v_mfma_f32_32x32x16_bf16 v[186:201], v[70:73], a[60:63], v[186:201]\n"
"ds_read_b64_tr_b16 v[54:55], v28 offset:12352\n"
"ds_read_b64_tr_b16 v[56:57], v28 offset:13120\n"
"v_rcp_f32 v205, v205\n"
"v_mul_f32 v122, v122, v202\n"
"v_mul_f32 v123, v123, v203\n"
"v_mfma_f32_4x4x4_16b_bf16 v[82:85], v[42:43], v[78:79], v[82:85]\n"
"v_mfma_f32_4x4x4_16b_bf16 v[86:89], v[42:43], v[80:81], v[86:89]\n"
"ds_read_b64_tr_b16 v[58:59], v32 offset:12288\n"
"ds_read_b64_tr_b16 v[60:61], v32 offset:13056\n"
"s_waitcnt lgkmcnt(6)\n"
"v_mfma_f32_32x32x16_bf16 v[154:169], v[44:47], a[64:67], v[154:169]\n"
"ds_read_b64_tr_b16 v[62:63], v28 offset:13824\n"
"ds_read_b64_tr_b16 v[64:65], v28 offset:14592\n"
"v_mul_f32 v124, v124, v204\n"
"v_mul_f32 v125, v125, v205\n"
"v_cvt_pk_bf16_f32 v206, v122, v123\n"
"s_waitcnt lgkmcnt(6)\n"
"v_mfma_f32_32x32x16_bf16 v[170:185], v[48:51], a[64:67], v[170:185]\n"
"ds_read_b64_tr_b16 v[66:67], v28 offset:13856\n"
"ds_read_b64_tr_b16 v[68:69], v28 offset:14624\n"
"s_add_u32 m0, s67, 4096\n"
"s_add_u32 s3, s65, 262144\n"
"buffer_load_dwordx4 v39, s[56:59], s3 offen lds\n"
"v_cvt_pk_bf16_f32 v207, v124, v125\n"
"v_mul_f32 v202, v17, v126\n"
"s_waitcnt lgkmcnt(6)\n"
"v_mfma_f32_32x32x16_bf16 v[186:201], v[54:57], a[64:67], v[186:201]\n"
"ds_read_b64_tr_b16 v[70:71], v28 offset:13888\n"
"ds_read_b64_tr_b16 v[72:73], v28 offset:14656\n"
"v_mul_f32 v203, v17, v127\n"
"v_mul_f32 v204, v17, v128\n"
"v_mul_f32 v205, v17, v129\n"
"s_waitcnt lgkmcnt(7)\n"
"v_mfma_f32_4x4x4_16b_bf16 v[82:85], v[42:43], v[58:59], v[82:85]\n"
"s_waitcnt lgkmcnt(6)\n"
"v_mfma_f32_4x4x4_16b_bf16 v[86:89], v[42:43], v[60:61], v[86:89]\n"
"ds_read_b64_tr_b16 v[78:79], v32 offset:13824\n"
"ds_read_b64_tr_b16 v[80:81], v32 offset:14592\n"
"s_waitcnt lgkmcnt(6)\n"
"v_mfma_f32_32x32x16_bf16 v[154:169], v[62:65], a[68:71], v[154:169]\n"
"ds_read_b64_tr_b16 v[44:45], v28 offset:15360\n"
"ds_read_b64_tr_b16 v[46:47], v28 offset:16128\n"
"v_exp_f32 v202, v202\n"
"v_mul_f32 v126, v126, v134\n"
"v_exp_f32 v203, v203\n"
"s_waitcnt lgkmcnt(6)\n"
"v_mfma_f32_32x32x16_bf16 v[170:185], v[66:69], a[68:71], v[170:185]\n"
"ds_read_b64_tr_b16 v[48:49], v28 offset:15392\n"
"ds_read_b64_tr_b16 v[50:51], v28 offset:16160\n"
"buffer_load_dwordx4 v40, s[56:59], s3 offen offset:1024 lds\n"
"v_mul_f32 v127, v127, v135\n"
"v_exp_f32 v204, v204\n"
"s_waitcnt lgkmcnt(6)\n"
"v_mfma_f32_32x32x16_bf16 v[186:201], v[70:73], a[68:71], v[186:201]\n"
"ds_read_b64_tr_b16 v[54:55], v28 offset:15424\n"
"ds_read_b64_tr_b16 v[56:57], v28 offset:16192\n"
"v_mul_f32 v128, v128, v136\n"
"v_exp_f32 v205, v205\n"
"v_mul_f32 v129, v129, v137\n"
"s_waitcnt lgkmcnt(7)\n"
"v_mfma_f32_4x4x4_16b_bf16 v[82:85], v[42:43], v[78:79], v[82:85]\n"
"s_waitcnt lgkmcnt(6)\n"
"v_mfma_f32_4x4x4_16b_bf16 v[86:89], v[42:43], v[80:81], v[86:89]\n"
"ds_read_b64_tr_b16 v[58:59], v32 offset:15360\n"
"ds_read_b64_tr_b16 v[60:61], v32 offset:16128\n"
"s_waitcnt lgkmcnt(6)\n"
"v_mfma_f32_32x32x16_bf16 v[154:169], v[44:47], a[72:75], v[154:169]\n"
"ds_read_b64_tr_b16 v[62:63], v28 offset:16896\n"
"ds_read_b64_tr_b16 v[64:65], v28 offset:17664\n"
"v_add_f32 v202, 1.0, v202\n"
"v_add_f32 v203, 1.0, v203\n"
"v_add_f32 v204, 1.0, v204\n"
"s_waitcnt lgkmcnt(6)\n"
"v_mfma_f32_32x32x16_bf16 v[170:185], v[48:51], a[72:75], v[170:185]\n"
"ds_read_b64_tr_b16 v[66:67], v28 offset:16928\n"
"ds_read_b64_tr_b16 v[68:69], v28 offset:17696\n"
"s_add_u32 m0, s68, 0\n"
"s_add_u32 s3, s65, 278528\n"
"buffer_load_dwordx4 v39, s[56:59], s3 offen lds\n"
"v_add_f32 v205, 1.0, v205\n"
"v_rcp_f32 v202, v202\n"
"s_waitcnt lgkmcnt(6)\n"
"v_mfma_f32_32x32x16_bf16 v[186:201], v[54:57], a[72:75], v[186:201]\n"
"ds_read_b64_tr_b16 v[70:71], v28 offset:16960\n"
"ds_read_b64_tr_b16 v[72:73], v28 offset:17728\n"
"v_rcp_f32 v203, v203\n"
"v_rcp_f32 v204, v204\n"
"v_rcp_f32 v205, v205\n"
"s_waitcnt lgkmcnt(7)\n"
"v_mfma_f32_4x4x4_16b_bf16 v[82:85], v[42:43], v[58:59], v[82:85]\n"
"s_waitcnt lgkmcnt(6)\n"
"v_mfma_f32_4x4x4_16b_bf16 v[86:89], v[42:43], v[60:61], v[86:89]\n"
"ds_read_b64_tr_b16 v[78:79], v32 offset:16896\n"
"ds_read_b64_tr_b16 v[80:81], v32 offset:17664\n"
"s_waitcnt lgkmcnt(6)\n"
"v_mfma_f32_32x32x16_bf16 v[154:169], v[62:65], a[76:79], v[154:169]\n"
"ds_read_b64_tr_b16 v[44:45], v28 offset:18432\n"
"ds_read_b64_tr_b16 v[46:47], v28 offset:19200\n"
"v_mul_f32 v126, v126, v202\n"
"v_mul_f32 v127, v127, v203\n"
"v_mul_f32 v128, v128, v204\n"
"s_waitcnt lgkmcnt(6)\n"
"v_mfma_f32_32x32x16_bf16 v[170:185], v[66:69], a[76:79], v[170:185]\n"
"ds_read_b64_tr_b16 v[48:49], v28 offset:18464\n"
"ds_read_b64_tr_b16 v[50:51], v28 offset:19232\n"
"buffer_load_dwordx4 v40, s[56:59], s3 offen offset:1024 lds\n"
"v_mul_f32 v129, v129, v205\n"
"v_cvt_pk_bf16_f32 v208, v126, v127\n"
"s_waitcnt lgkmcnt(6)\n"
"v_mfma_f32_32x32x16_bf16 v[186:201], v[70:73], a[76:79], v[186:201]\n"
"ds_read_b64_tr_b16 v[54:55], v28 offset:18496\n"
"ds_read_b64_tr_b16 v[56:57], v28 offset:19264\n"
"v_cvt_pk_bf16_f32 v209, v128, v129\n"
"buffer_store_dwordx4 v[206:209], v4, s[8:11], s19 offen offset:2048 sc1\n"
"v_mul_f32 v202, v17, v138\n"
"s_waitcnt lgkmcnt(7)\n"
"v_mfma_f32_4x4x4_16b_bf16 v[82:85], v[42:43], v[78:79], v[82:85]\n"
"s_waitcnt lgkmcnt(6)\n"
"v_mfma_f32_4x4x4_16b_bf16 v[86:89], v[42:43], v[80:81], v[86:89]\n"
"ds_read_b64_tr_b16 v[58:59], v32 offset:18432\n"
"ds_read_b64_tr_b16 v[60:61], v32 offset:19200\n"
"s_waitcnt lgkmcnt(6)\n"
"v_mfma_f32_32x32x16_bf16 v[154:169], v[44:47], a[80:83], v[154:169]\n"
"ds_read_b64_tr_b16 v[62:63], v28 offset:19968\n"
"ds_read_b64_tr_b16 v[64:65], v28 offset:20736\n"
"v_mul_f32 v203, v17, v139\n"
"v_mul_f32 v204, v17, v140\n"
"v_mul_f32 v205, v17, v141\n"
"s_waitcnt lgkmcnt(6)\n"
"v_mfma_f32_32x32x16_bf16 v[170:185], v[48:51], a[80:83], v[170:185]\n"
"ds_read_b64_tr_b16 v[66:67], v28 offset:20000\n"
"ds_read_b64_tr_b16 v[68:69], v28 offset:20768\n"
"s_add_u32 m0, s68, 2048\n"
"s_add_u32 s3, s65, 294912\n"
"buffer_load_dwordx4 v39, s[56:59], s3 offen lds\n"
"v_exp_f32 v202, v202\n"
"v_mul_f32 v138, v138, v146\n"
"s_waitcnt lgkmcnt(6)\n"
"v_mfma_f32_32x32x16_bf16 v[186:201], v[54:57], a[80:83], v[186:201]\n"
"ds_read_b64_tr_b16 v[70:71], v28 offset:20032\n"
"ds_read_b64_tr_b16 v[72:73], v28 offset:20800\n"
"v_exp_f32 v203, v203\n"
"v_mul_f32 v139, v139, v147\n"
"v_exp_f32 v204, v204\n"
"s_waitcnt lgkmcnt(7)\n"
"v_mfma_f32_4x4x4_16b_bf16 v[82:85], v[42:43], v[58:59], v[82:85]\n"
"s_waitcnt lgkmcnt(6)\n"
"v_mfma_f32_4x4x4_16b_bf16 v[86:89], v[42:43], v[60:61], v[86:89]\n"
"ds_read_b64_tr_b16 v[78:79], v32 offset:19968\n"
"ds_read_b64_tr_b16 v[80:81], v32 offset:20736\n"
"s_waitcnt lgkmcnt(6)\n"
"v_mfma_f32_32x32x16_bf16 v[154:169], v[62:65], a[84:87], v[154:169]\n"
"ds_read_b64_tr_b16 v[44:45], v28 offset:21504\n"
"ds_read_b64_tr_b16 v[46:47], v28 offset:22272\n"
"v_mul_f32 v140, v140, v148\n"
"v_exp_f32 v205, v205\n"
"v_mul_f32 v141, v141, v149\n"
"s_waitcnt lgkmcnt(6)\n"
"v_mfma_f32_32x32x16_bf16 v[170:185], v[66:69], a[84:87], v[170:185]\n"
"ds_read_b64_tr_b16 v[48:49], v28 offset:21536\n"
"ds_read_b64_tr_b16 v[50:51], v28 offset:22304\n"
"buffer_load_dwordx4 v40, s[56:59], s3 offen offset:1024 lds\n"
"v_add_f32 v202, 1.0, v202\n"
"v_add_f32 v203, 1.0, v203\n"
"s_waitcnt lgkmcnt(6)\n"
"v_mfma_f32_32x32x16_bf16 v[186:201], v[70:73], a[84:87], v[186:201]\n"
"ds_read_b64_tr_b16 v[54:55], v28 offset:21568\n"
"ds_read_b64_tr_b16 v[56:57], v28 offset:22336\n"
"v_add_f32 v204, 1.0, v204\n"
"v_add_f32 v205, 1.0, v205\n"
"v_rcp_f32 v202, v202\n"
"s_waitcnt lgkmcnt(7)\n"
"v_mfma_f32_4x4x4_16b_bf16 v[82:85], v[42:43], v[78:79], v[82:85]\n"
"s_waitcnt lgkmcnt(6)\n"
"v_mfma_f32_4x4x4_16b_bf16 v[86:89], v[42:43], v[80:81], v[86:89]\n"
"ds_read_b64_tr_b16 v[58:59], v32 offset:21504\n"
"ds_read_b64_tr_b16 v[60:61], v32 offset:22272\n"
"s_waitcnt lgkmcnt(6)\n"
"v_mfma_f32_32x32x16_bf16 v[154:169], v[44:47], a[88:91], v[154:169]\n"
"ds_read_b64_tr_b16 v[62:63], v28 offset:23040\n"
"ds_read_b64_tr_b16 v[64:65], v28 offset:23808\n"
"v_rcp_f32 v203, v203\n"
"v_rcp_f32 v204, v204\n"
"v_rcp_f32 v205, v205\n"
"s_waitcnt lgkmcnt(6)\n"
"v_mfma_f32_32x32x16_bf16 v[170:185], v[48:51], a[88:91], v[170:185]\n"
"ds_read_b64_tr_b16 v[66:67], v28 offset:23072\n"
"ds_read_b64_tr_b16 v[68:69], v28 offset:23840\n"
"s_add_u32 m0, s68, 4096\n"
"s_add_u32 s3, s65, 311296\n"
"buffer_load_dwordx4 v39, s[56:59], s3 offen lds\n"
"v_mul_f32 v138, v138, v202\n"
"v_mul_f32 v139, v139, v203\n"
"s_waitcnt lgkmcnt(6)\n"
"v_mfma_f32_32x32x16_bf16 v[186:201], v[54:57], a[88:91], v[186:201]\n"
"ds_read_b64_tr_b16 v[70:71], v28 offset:23104\n"
"ds_read_b64_tr_b16 v[72:73], v28 offset:23872\n"
"v_mul_f32 v140, v140, v204\n"
"v_mul_f32 v141, v141, v205\n"
"v_cvt_pk_bf16_f32 v210, v138, v139\n"
"s_waitcnt lgkmcnt(7)\n"
"v_mfma_f32_4x4x4_16b_bf16 v[82:85], v[42:43], v[58:59], v[82:85]\n"
"s_waitcnt lgkmcnt(6)\n"
"v_mfma_f32_4x4x4_16b_bf16 v[86:89], v[42:43], v[60:61], v[86:89]\n"
"ds_read_b64_tr_b16 v[78:79], v32 offset:23040\n"
"ds_read_b64_tr_b16 v[80:81], v32 offset:23808\n"
"s_waitcnt lgkmcnt(6)\n"
"v_mfma_f32_32x32x16_bf16 v[154:169], v[62:65], a[92:95], v[154:169]\n"
"s_waitcnt vmcnt(26)\n"
"s_waitcnt lgkmcnt(0)\n"
"s_barrier\n"
"ds_read_b64_tr_b16 v[44:45], v28 offset:24576\n"
"ds_read_b64_tr_b16 v[46:47], v28 offset:25344\n"
"v_cvt_pk_bf16_f32 v211, v140, v141\n"
"v_mul_f32 v202, v17, v142\n"
"v_mul_f32 v203, v17, v143\n"
"v_mfma_f32_32x32x16_bf16 v[170:185], v[66:69], a[92:95], v[170:185]\n"
"ds_read_b64_tr_b16 v[48:49], v28 offset:24608\n"
"ds_read_b64_tr_b16 v[50:51], v28 offset:25376\n"
"buffer_load_dwordx4 v40, s[56:59], s3 offen offset:1024 lds\n"
"v_mul_f32 v204, v17, v144\n"
"v_mul_f32 v205, v17, v145\n"
"v_mfma_f32_32x32x16_bf16 v[186:201], v[70:73], a[92:95], v[186:201]\n"
"ds_read_b64_tr_b16 v[54:55], v28 offset:24640\n"
"ds_read_b64_tr_b16 v[56:57], v28 offset:25408\n"
"v_exp_f32 v202, v202\n"
"v_mul_f32 v142, v142, v150\n"
"v_exp_f32 v203, v203\n"
"v_mfma_f32_4x4x4_16b_bf16 v[82:85], v[42:43], v[78:79], v[82:85]\n"
"v_mfma_f32_4x4x4_16b_bf16 v[86:89], v[42:43], v[80:81], v[86:89]\n"
"ds_read_b64_tr_b16 v[58:59], v32 offset:24576\n"
"ds_read_b64_tr_b16 v[60:61], v32 offset:25344\n"
"s_waitcnt lgkmcnt(6)\n"
"v_mfma_f32_32x32x16_bf16 v[154:169], v[44:47], a[96:99], v[154:169]\n"
"ds_read_b64_tr_b16 v[62:63], v28 offset:26112\n"
"ds_read_b64_tr_b16 v[64:65], v28 offset:26880\n"
"v_mul_f32 v143, v143, v151\n"
"v_exp_f32 v204, v204\n"
"v_mul_f32 v144, v144, v152\n"
"s_waitcnt lgkmcnt(6)\n"
"v_mfma_f32_32x32x16_bf16 v[170:185], v[48:51], a[96:99], v[170:185]\n"
"ds_read_b64_tr_b16 v[66:67], v28 offset:26144\n"
"ds_read_b64_tr_b16 v[68:69], v28 offset:26912\n"
"s_add_u32 m0, s68, 12288\n"
"s_add_u32 s3, s65, 393216\n"
"buffer_load_dwordx4 v39, s[56:59], s3 offen lds\n"
"v_exp_f32 v205, v205\n"
"v_mul_f32 v145, v145, v153\n"
"s_waitcnt lgkmcnt(6)\n"
"v_mfma_f32_32x32x16_bf16 v[186:201], v[54:57], a[96:99], v[186:201]\n"
"ds_read_b64_tr_b16 v[70:71], v28 offset:26176\n"
"ds_read_b64_tr_b16 v[72:73], v28 offset:26944\n"
"v_add_f32 v202, 1.0, v202\n"
"v_add_f32 v203, 1.0, v203\n"
"v_add_f32 v204, 1.0, v204\n"
"s_waitcnt lgkmcnt(7)\n"
"v_mfma_f32_4x4x4_16b_bf16 v[82:85], v[42:43], v[58:59], v[82:85]\n"
"s_waitcnt lgkmcnt(6)\n"
"v_mfma_f32_4x4x4_16b_bf16 v[86:89], v[42:43], v[60:61], v[86:89]\n"
"ds_read_b64_tr_b16 v[78:79], v32 offset:26112\n"
"ds_read_b64_tr_b16 v[80:81], v32 offset:26880\n"
"s_waitcnt lgkmcnt(6)\n"
"v_mfma_f32_32x32x16_bf16 v[154:169], v[62:65], a[100:103], v[154:169]\n"
"ds_read_b64_tr_b16 v[44:45], v28 offset:27648\n"
"ds_read_b64_tr_b16 v[46:47], v28 offset:28416\n"
"v_add_f32 v205, 1.0, v205\n"
"v_rcp_f32 v202, v202\n"
"v_rcp_f32 v203, v203\n"
"s_waitcnt lgkmcnt(6)\n"
"v_mfma_f32_32x32x16_bf16 v[170:185], v[66:69], a[100:103], v[170:185]\n"
"ds_read_b64_tr_b16 v[48:49], v28 offset:27680\n"
"ds_read_b64_tr_b16 v[50:51], v28 offset:28448\n"
"buffer_load_dwordx4 v40, s[56:59], s3 offen offset:1024 lds\n"
"v_rcp_f32 v204, v204\n"
"v_rcp_f32 v205, v205\n"
"s_waitcnt lgkmcnt(6)\n"
"v_mfma_f32_32x32x16_bf16 v[186:201], v[70:73], a[100:103], v[186:201]\n"
"ds_read_b64_tr_b16 v[54:55], v28 offset:27712\n"
"ds_read_b64_tr_b16 v[56:57], v28 offset:28480\n"
"v_mul_f32 v142, v142, v202\n"
"v_mul_f32 v143, v143, v203\n"
"v_mul_f32 v144, v144, v204\n"
"s_waitcnt lgkmcnt(7)\n"
"v_mfma_f32_4x4x4_16b_bf16 v[82:85], v[42:43], v[78:79], v[82:85]\n"
"s_waitcnt lgkmcnt(6)\n"
"v_mfma_f32_4x4x4_16b_bf16 v[86:89], v[42:43], v[80:81], v[86:89]\n"
"ds_read_b64_tr_b16 v[58:59], v32 offset:27648\n"
"ds_read_b64_tr_b16 v[60:61], v32 offset:28416\n"
"s_waitcnt lgkmcnt(6)\n"
"v_mfma_f32_32x32x16_bf16 v[154:169], v[44:47], a[104:107], v[154:169]\n"
"ds_read_b64_tr_b16 v[62:63], v28 offset:29184\n"
"ds_read_b64_tr_b16 v[64:65], v28 offset:29952\n"
"v_mul_f32 v145, v145, v205\n"
"v_cvt_pk_bf16_f32 v212, v142, v143\n"
"v_cvt_pk_bf16_f32 v213, v144, v145\n"
"s_waitcnt lgkmcnt(6)\n"
"v_mfma_f32_32x32x16_bf16 v[170:185], v[48:51], a[104:107], v[170:185]\n"
"ds_read_b64_tr_b16 v[66:67], v28 offset:29216\n"
"ds_read_b64_tr_b16 v[68:69], v28 offset:29984\n"
"s_add_u32 m0, s68, 14336\n"
"s_add_u32 s3, s65, 409600\n"
"buffer_load_dwordx4 v39, s[56:59], s3 offen lds\n"
"buffer_store_dwordx4 v[210:213], v4, s[8:11], s19 offen offset:3072 sc1\n"
"s_waitcnt vmcnt(0)\n"
"s_waitcnt lgkmcnt(6)\n"
"v_mfma_f32_32x32x16_bf16 v[186:201], v[54:57], a[104:107], v[186:201]\n"
"ds_read_b64_tr_b16 v[70:71], v28 offset:29248\n"
"ds_read_b64_tr_b16 v[72:73], v28 offset:30016\n"
"s_waitcnt lgkmcnt(7)\n"
"v_mfma_f32_4x4x4_16b_bf16 v[82:85], v[42:43], v[58:59], v[82:85]\n"
"s_waitcnt lgkmcnt(6)\n"
"v_mfma_f32_4x4x4_16b_bf16 v[86:89], v[42:43], v[60:61], v[86:89]\n"
"ds_read_b64_tr_b16 v[78:79], v32 offset:29184\n"
"ds_read_b64_tr_b16 v[80:81], v32 offset:29952\n"
"s_waitcnt lgkmcnt(6)\n"
"v_mfma_f32_32x32x16_bf16 v[154:169], v[62:65], a[108:111], v[154:169]\n"
"ds_read_b64_tr_b16 v[44:45], v28 offset:30720\n"
"ds_read_b64_tr_b16 v[46:47], v28 offset:31488\n"
"s_waitcnt lgkmcnt(6)\n"
"v_mfma_f32_32x32x16_bf16 v[170:185], v[66:69], a[108:111], v[170:185]\n"
"ds_read_b64_tr_b16 v[48:49], v28 offset:30752\n"
"ds_read_b64_tr_b16 v[50:51], v28 offset:31520\n"
"buffer_load_dwordx4 v40, s[56:59], s3 offen offset:1024 lds\n"
"s_waitcnt lgkmcnt(6)\n"
"v_mfma_f32_32x32x16_bf16 v[186:201], v[70:73], a[108:111], v[186:201]\n"
"ds_read_b64_tr_b16 v[54:55], v28 offset:30784\n"
"ds_read_b64_tr_b16 v[56:57], v28 offset:31552\n"
"s_waitcnt lgkmcnt(7)\n"
"v_mfma_f32_4x4x4_16b_bf16 v[82:85], v[42:43], v[78:79], v[82:85]\n"
"s_waitcnt lgkmcnt(6)\n"
"v_mfma_f32_4x4x4_16b_bf16 v[86:89], v[42:43], v[80:81], v[86:89]\n"
"ds_read_b64_tr_b16 v[58:59], v32 offset:30720\n"
"ds_read_b64_tr_b16 v[60:61], v32 offset:31488\n"
"s_waitcnt lgkmcnt(6)\n"
"v_mfma_f32_32x32x16_bf16 v[154:169], v[44:47], a[112:115], v[154:169]\n"
"ds_read_b64_tr_b16 v[62:63], v28 offset:32256\n"
"ds_read_b64_tr_b16 v[64:65], v28 offset:33024\n"
"buffer_store_dwordx2 v[76:77], v53, s[8:11], s23 offen sc1\n"
"s_waitcnt lgkmcnt(6)\n"
"v_mfma_f32_32x32x16_bf16 v[170:185], v[48:51], a[112:115], v[170:185]\n"
"ds_read_b64_tr_b16 v[66:67], v28 offset:32288\n"
"ds_read_b64_tr_b16 v[68:69], v28 offset:33056\n"
"s_add_u32 m0, s68, 16384\n"
"s_add_u32 s3, s65, 425984\n"
"buffer_load_dwordx4 v39, s[56:59], s3 offen lds\n"
"s_waitcnt lgkmcnt(6)\n"
"v_mfma_f32_32x32x16_bf16 v[186:201], v[54:57], a[112:115], v[186:201]\n"
"ds_read_b64_tr_b16 v[70:71], v28 offset:32320\n"
"ds_read_b64_tr_b16 v[72:73], v28 offset:33088\n"
"s_waitcnt lgkmcnt(7)\n"
"v_mfma_f32_4x4x4_16b_bf16 v[82:85], v[42:43], v[58:59], v[82:85]\n"
"s_waitcnt lgkmcnt(6)\n"
"v_mfma_f32_4x4x4_16b_bf16 v[86:89], v[42:43], v[60:61], v[86:89]\n"
"ds_read_b64_tr_b16 v[78:79], v32 offset:32256\n"
"ds_read_b64_tr_b16 v[80:81], v32 offset:33024\n"
"s_waitcnt lgkmcnt(6)\n"
"v_mfma_f32_32x32x16_bf16 v[154:169], v[62:65], a[116:119], v[154:169]\n"
"ds_read_b64_tr_b16 v[44:45], v28 offset:33792\n"
"ds_read_b64_tr_b16 v[46:47], v28 offset:34560\n"
"s_waitcnt lgkmcnt(6)\n"
"v_mfma_f32_32x32x16_bf16 v[170:185], v[66:69], a[116:119], v[170:185]\n"
"ds_read_b64_tr_b16 v[48:49], v28 offset:33824\n"
"ds_read_b64_tr_b16 v[50:51], v28 offset:34592\n"
"buffer_load_dwordx4 v40, s[56:59], s3 offen offset:1024 lds\n"
"s_waitcnt lgkmcnt(6)\n"
"v_mfma_f32_32x32x16_bf16 v[186:201], v[70:73], a[116:119], v[186:201]\n"
"ds_read_b64_tr_b16 v[54:55], v28 offset:33856\n"
"ds_read_b64_tr_b16 v[56:57], v28 offset:34624\n"
"s_waitcnt lgkmcnt(7)\n"
"v_mfma_f32_4x4x4_16b_bf16 v[82:85], v[42:43], v[78:79], v[82:85]\n"
"s_waitcnt lgkmcnt(6)\n"
"v_mfma_f32_4x4x4_16b_bf16 v[86:89], v[42:43], v[80:81], v[86:89]\n"
"ds_read_b64_tr_b16 v[58:59], v32 offset:33792\n"
"ds_read_b64_tr_b16 v[60:61], v32 offset:34560\n"
"s_waitcnt lgkmcnt(6)\n"
"v_mfma_f32_32x32x16_bf16 v[154:169], v[44:47], a[120:123], v[154:169]\n"
"ds_read_b64_tr_b16 v[62:63], v28 offset:35328\n"
"ds_read_b64_tr_b16 v[64:65], v28 offset:36096\n"
"s_waitcnt lgkmcnt(6)\n"
"v_mfma_f32_32x32x16_bf16 v[170:185], v[48:51], a[120:123], v[170:185]\n"
"ds_read_b64_tr_b16 v[66:67], v28 offset:35360\n"
"ds_read_b64_tr_b16 v[68:69], v28 offset:36128\n"
"s_waitcnt lgkmcnt(6)\n"
"v_mfma_f32_32x32x16_bf16 v[186:201], v[54:57], a[120:123], v[186:201]\n"
"ds_read_b64_tr_b16 v[70:71], v28 offset:35392\n"
"ds_read_b64_tr_b16 v[72:73], v28 offset:36160\n"
"s_waitcnt lgkmcnt(7)\n"
"v_mfma_f32_4x4x4_16b_bf16 v[82:85], v[42:43], v[58:59], v[82:85]\n"
"s_waitcnt lgkmcnt(6)\n"
"v_mfma_f32_4x4x4_16b_bf16 v[86:89], v[42:43], v[60:61], v[86:89]\n"
"ds_read_b64_tr_b16 v[78:79], v32 offset:35328\n"
"ds_read_b64_tr_b16 v[80:81], v32 offset:36096\n"
"s_waitcnt lgkmcnt(6)\n"
"v_mfma_f32_32x32x16_bf16 v[154:169], v[62:65], a[124:127], v[154:169]\n"
"s_waitcnt lgkmcnt(4)\n"
"v_mfma_f32_32x32x16_bf16 v[170:185], v[66:69], a[124:127], v[170:185]\n"
"s_waitcnt lgkmcnt(2)\n"
"v_mfma_f32_32x32x16_bf16 v[186:201], v[70:73], a[124:127], v[186:201]\n"
"s_waitcnt lgkmcnt(1)\n"
"v_mfma_f32_4x4x4_16b_bf16 v[82:85], v[42:43], v[78:79], v[82:85]\n"
"s_waitcnt lgkmcnt(0)\n"
"v_mfma_f32_4x4x4_16b_bf16 v[86:89], v[42:43], v[80:81], v[86:89]\n"
"s_nop 4\n"
"v_add_f32 v82, v82, v86\n"
"ds_write_b32 v36, v82 offset:1024\n"
"s_waitcnt lgkmcnt(0)\n"
"s_barrier\n"
"ds_read_b32 v20, v37 offset:1024\n"
"ds_read_b32 v21, v37 offset:1280\n"
"ds_read_b32 v22, v37 offset:1536\n"
"s_lshl2_add_u32 s40, s61, s15\n"
"s_lshl3_add_u32 s40, s40, s51\n"
"s_lshl_b32 s40, s40, 8\n"
"s_add_u32 s7, s40, 266240\n"
"v_bfe_u32 v5, v1, 1, 2\n"
"v_and_b32 v6, 1, v1\n"
"v_lshlrev_b32 v6, 7, v6\n"
"v_lshl_add_u32 v18, v5, 9, v6\n"
"buffer_load_dwordx2 v[34:35], v18, s[8:11], s7 offen sc1\n"
"s_waitcnt lgkmcnt(2)\n"
"v_mfma_f32_32x32x2_f32 v[154:169], v20, v16, v[154:169]\n"
"s_waitcnt lgkmcnt(1)\n"
"v_mfma_f32_32x32x2_f32 v[170:185], v21, v16, v[170:185]\n"
"s_waitcnt lgkmcnt(0)\n"
"v_mfma_f32_32x32x2_f32 v[186:201], v22, v16, v[186:201]\n"
"s_add_u32 m0, s68, 24576\n"
"s_add_u32 s3, s65, 442368\n"
"buffer_load_dwordx4 v39, s[56:59], s3 offen lds\n"
"buffer_load_dwordx4 v40, s[56:59], s3 offen offset:1024 lds\n"
"s_nop 9\n"
"v_mul_f32 v202, v17, v154\n"
"v_mul_f32 v203, v17, v155\n"
"v_mul_f32 v204, v17, v156\n"
"v_mul_f32 v205, v17, v157\n"
"v_exp_f32 v202, v202\n"
"v_mul_f32 v154, v154, v162\n"
"v_exp_f32 v203, v203\n"
"v_mul_f32 v155, v155, v163\n"
"v_exp_f32 v204, v204\n"
"v_mul_f32 v156, v156, v164\n"
"v_exp_f32 v205, v205\n"
"v_mul_f32 v157, v157, v165\n"
"v_add_f32 v202, 1.0, v202\n"
"v_add_f32 v203, 1.0, v203\n"
"v_add_f32 v204, 1.0, v204\n"
"v_add_f32 v205, 1.0, v205\n"
"v_rcp_f32 v202, v202\n"
"v_rcp_f32 v203, v203\n"
"v_rcp_f32 v204, v204\n"
"v_rcp_f32 v205, v205\n"
"v_mul_f32 v154, v154, v202\n"
"v_mul_f32 v155, v155, v203\n"
"v_mul_f32 v156, v156, v204\n"
"v_mul_f32 v157, v157, v205\n"
"v_cvt_pk_bf16_f32 v206, v154, v155\n"
"v_cvt_pk_bf16_f32 v207, v156, v157\n"
"v_mul_f32 v202, v17, v158\n"
"v_mul_f32 v203, v17, v159\n"
"v_mul_f32 v204, v17, v160\n"
"v_mul_f32 v205, v17, v161\n"
"v_exp_f32 v202, v202\n"
"v_mul_f32 v158, v158, v166\n"
"v_exp_f32 v203, v203\n"
"v_mul_f32 v159, v159, v167\n"
"v_exp_f32 v204, v204\n"
"v_mul_f32 v160, v160, v168\n"
"v_exp_f32 v205, v205\n"
"v_mul_f32 v161, v161, v169\n"
"v_add_f32 v202, 1.0, v202\n"
"v_add_f32 v203, 1.0, v203\n"
"v_add_f32 v204, 1.0, v204\n"
"v_add_f32 v205, 1.0, v205\n"
"v_rcp_f32 v202, v202\n"
"v_rcp_f32 v203, v203\n"
"v_rcp_f32 v204, v204\n"
"v_rcp_f32 v205, v205\n"
"v_mul_f32 v158, v158, v202\n"
"v_mul_f32 v159, v159, v203\n"
"v_mul_f32 v160, v160, v204\n"
"v_mul_f32 v161, v161, v205\n"
"v_cvt_pk_bf16_f32 v208, v158, v159\n"
"v_cvt_pk_bf16_f32 v209, v160, v161\n"
"buffer_store_dwordx4 v[206:209], v4, s[8:11], s20 offen sc1\n"
"s_add_u32 m0, s68, 26624\n"
"s_add_u32 s3, s65, 65536\n"
"buffer_load_dwordx4 v39, s[56:59], s3 offen lds\n"
"buffer_load_dwordx4 v40, s[56:59], s3 offen offset:1024 lds\n"
"v_mul_f32 v202, v17, v170\n"
"v_mul_f32 v203, v17, v171\n"
"v_mul_f32 v204, v17, v172\n"
"v_mul_f32 v205, v17, v173\n"
"v_exp_f32 v202, v202\n"
"v_mul_f32 v170, v170, v178\n"
"v_exp_f32 v203, v203\n"
"v_mul_f32 v171, v171, v179\n"
"v_exp_f32 v204, v204\n"
"v_mul_f32 v172, v172, v180\n"
"v_exp_f32 v205, v205\n"
"v_mul_f32 v173, v173, v181\n"
"v_add_f32 v202, 1.0, v202\n"
"v_add_f32 v203, 1.0, v203\n"
"v_add_f32 v204, 1.0, v204\n"
"v_add_f32 v205, 1.0, v205\n"
"v_rcp_f32 v202, v202\n"
"v_rcp_f32 v203, v203\n"
"v_rcp_f32 v204, v204\n"
"v_rcp_f32 v205, v205\n"
"v_mul_f32 v170, v170, v202\n"
"v_mul_f32 v171, v171, v203\n"
"v_mul_f32 v172, v172, v204\n"
"v_mul_f32 v173, v173, v205\n"
"v_cvt_pk_bf16_f32 v210, v170, v171\n"
"v_cvt_pk_bf16_f32 v211, v172, v173\n"
"v_mul_f32 v202, v17, v174\n"
"v_mul_f32 v203, v17, v175\n"
"v_mul_f32 v204, v17, v176\n"
"v_mul_f32 v205, v17, v177\n"
"v_exp_f32 v202, v202\n"
"v_mul_f32 v174, v174, v182\n"
"v_exp_f32 v203, v203\n"
"v_mul_f32 v175, v175, v183\n"
"v_exp_f32 v204, v204\n"
"v_mul_f32 v176, v176, v184\n"
"v_exp_f32 v205, v205\n"
"v_mul_f32 v177, v177, v185\n"
"v_add_f32 v202, 1.0, v202\n"
"v_add_f32 v203, 1.0, v203\n"
"v_add_f32 v204, 1.0, v204\n"
"v_add_f32 v205, 1.0, v205\n"
"v_rcp_f32 v202, v202\n"
"v_rcp_f32 v203, v203\n"
"v_rcp_f32 v204, v204\n"
"v_rcp_f32 v205, v205\n"
"v_mul_f32 v174, v174, v202\n"
"v_mul_f32 v175, v175, v203\n"
"v_mul_f32 v176, v176, v204\n"
"v_mul_f32 v177, v177, v205\n"
"v_cvt_pk_bf16_f32 v212, v174, v175\n"
"v_cvt_pk_bf16_f32 v213, v176, v177\n"
"buffer_store_dwordx4 v[210:213], v4, s[8:11], s21 offen sc1\n"
"s_add_u32 m0, s68, 28672\n"
"s_add_u32 s3, s65, 81920\n"
"buffer_load_dwordx4 v39, s[56:59], s3 offen lds\n"
"buffer_load_dwordx4 v40, s[56:59], s3 offen offset:1024 lds\n"
"v_mul_f32 v202, v17, v186\n"
"v_mul_f32 v203, v17, v187\n"
"v_mul_f32 v204, v17, v188\n"
"v_mul_f32 v205, v17, v189\n"
"v_exp_f32 v202, v202\n"
"v_mul_f32 v186, v186, v194\n"
"v_exp_f32 v203, v203\n"
"v_mul_f32 v187, v187, v195\n"
"v_exp_f32 v204, v204\n"
"v_mul_f32 v188, v188, v196\n"
"v_exp_f32 v205, v205\n"
"v_mul_f32 v189, v189, v197\n"
"v_add_f32 v202, 1.0, v202\n"
"v_add_f32 v203, 1.0, v203\n"
"v_add_f32 v204, 1.0, v204\n"
"v_add_f32 v205, 1.0, v205\n"
"v_rcp_f32 v202, v202\n"
"v_rcp_f32 v203, v203\n"
"v_rcp_f32 v204, v204\n"
"v_rcp_f32 v205, v205\n"
"v_mul_f32 v186, v186, v202\n"
"v_mul_f32 v187, v187, v203\n"
"v_mul_f32 v188, v188, v204\n"
"v_mul_f32 v189, v189, v205\n"
"v_cvt_pk_bf16_f32 v206, v186, v187\n"
"v_cvt_pk_bf16_f32 v207, v188, v189\n"
"v_mul_f32 v202, v17, v190\n"
"v_mul_f32 v203, v17, v191\n"
"v_mul_f32 v204, v17, v192\n"
"v_mul_f32 v205, v17, v193\n"
"v_exp_f32 v202, v202\n"
"v_mul_f32 v190, v190, v198\n"
"v_exp_f32 v203, v203\n"
"v_mul_f32 v191, v191, v199\n"
"v_exp_f32 v204, v204\n"
"v_mul_f32 v192, v192, v200\n"
"v_exp_f32 v205, v205\n"
"v_mul_f32 v193, v193, v201\n"
"v_add_f32 v202, 1.0, v202\n"
"v_add_f32 v203, 1.0, v203\n"
"v_add_f32 v204, 1.0, v204\n"
"v_add_f32 v205, 1.0, v205\n"
"v_rcp_f32 v202, v202\n"
"v_rcp_f32 v203, v203\n"
"v_rcp_f32 v204, v204\n"
"v_rcp_f32 v205, v205\n"
"v_mul_f32 v190, v190, v202\n"
"v_mul_f32 v191, v191, v203\n"
"v_mul_f32 v192, v192, v204\n"
"v_mul_f32 v193, v193, v205\n"
"v_cvt_pk_bf16_f32 v208, v190, v191\n"
"v_cvt_pk_bf16_f32 v209, v192, v193\n"
"buffer_store_dwordx4 v[206:209], v4, s[8:11], s22 offen sc1\n"
"s_load_dwordx2 s[16:17], %0, 48\n"
"s_mov_b32 s18, 2097152\n"
"s_mov_b32 s19, 0x20000\n"
"s_waitcnt vmcnt(9)\n"
"v_cmp_ne_u64 vcc, s[12:13], v[34:35]\n"
"s_nop 4\n"
"s_cbranch_vccnz .Lretry5_%=\n"
".Lback5_%=:\n"
"s_lshl1_add_u32 s40, s61, s51\n"
"s_lshl1_add_u32 s40, s40, s27\n"
"s_xor_b32 s41, s40, 1\n"
"s_mul_i32 s42, s15, 28672\n"
"s_add_u32 s42, s42, 9437184\n"
"s_mul_i32 s4, s40, 114688\n"
"s_add_u32 s4, s4, s42\n"
"s_mul_i32 s5, s41, 114688\n"
"s_add_u32 s5, s5, s42\n"
"v_add_u32 v13, s4, v4\n"
"v_add_u32 v14, s5, v4\n"
"s_mov_b32 s6, 0\n"
"buffer_load_dwordx4 a[128:131], v13, s[8:11], s6 offen offset:0 sc1\n"
"buffer_load_dwordx4 a[132:135], v14, s[8:11], s6 offen offset:0 sc1\n"
"buffer_load_dwordx4 a[136:139], v13, s[8:11], s6 offen offset:1024 sc1\n"
"buffer_load_dwordx4 a[140:143], v14, s[8:11], s6 offen offset:1024 sc1\n"
"buffer_load_dwordx4 a[144:147], v13, s[8:11], s6 offen offset:2048 sc1\n"
"buffer_load_dwordx4 a[148:151], v14, s[8:11], s6 offen offset:2048 sc1\n"
"buffer_load_dwordx4 a[152:155], v13, s[8:11], s6 offen offset:3072 sc1\n"
"buffer_load_dwordx4 a[156:159], v14, s[8:11], s6 offen offset:3072 sc1\n"
"s_waitcnt vmcnt(8)\n"
"s_add_u32 s41, s23, 131072\n"
"buffer_store_dwordx2 v[76:77], v53, s[8:11], s41 offen sc1\n"
"v_bfe_u32 v5, v1, 2, 2\n"
"v_lshl_add_u32 v6, v3, 2, v5\n"
"v_lshl_add_u32 v6, v6, 7, v19\n"
"v_lshrrev_b32 v5, 1, v5\n"
"v_xor_b32 v5, s60, v5\n"
"v_lshl_add_u32 v6, v5, 6, v6\n"
"v_xor_b32 v7, 64, v6\n"
"v_add_u32 v15, s66, v6\n"
"v_add_u32 v16, s66, v7\n"
"v_add_u32 v17, s67, v6\n"
"v_add_u32 v20, s67, v7\n"
"v_add_u32 v21, s68, v6\n"
"v_add_u32 v22, s68, v7\n"
"s_lshl3_add_u32 s41, s40, s24\n"
"s_lshl1_add_u32 s41, s41, s60\n"
"s_lshl_b32 s41, s41, 11\n"
"s_add_u32 s20, s41, 6291456\n"
"s_lshl_b32 s41, s40, 12\n"
"s_add_u32 s21, s41, 8388608\n"
"v_lshl_add_u32 v23, v3, 11, v24\n"
"s_lshl_b32 s41, s40, 15\n"
"s_lshl1_add_u32 s42, s24, s60\n"
"s_lshl_b32 s42, s42, 6\n"
"s_add_u32 s22, s41, s42\n"
"v_lshlrev_b32 v25, 10, v2\n"
"v_lshl_add_u32 v25, v3, 4, v25\n"
"s_add_u32 s26, s7, 131072\n"
"ds_read_b64_tr_b16 v[26:27], v15 offset:0\n"
"ds_read_b64_tr_b16 v[28:29], v15 offset:1024\n"
"ds_read_b64_tr_b16 v[30:31], v16 offset:0\n"
"ds_read_b64_tr_b16 v[32:33], v16 offset:1024\n"
"ds_read_b64_tr_b16 v[42:43], v15 offset:2048\n"
"ds_read_b64_tr_b16 v[44:45], v15 offset:3072\n"
"s_waitcnt vmcnt(8) lgkmcnt(4)\n"
"v_mfma_f32_32x32x16_bf16 v[54:69], v[26:29], a[128:131], 0\n"
"ds_read_b64_tr_b16 v[46:47], v16 offset:2048\n"
"ds_read_b64_tr_b16 v[48:49], v16 offset:3072\n"
"s_waitcnt vmcnt(7)\n"
"v_mfma_f32_32x32x16_bf16 v[78:93], v[26:29], a[132:135], 0\n"
"s_mov_b32 s6, 8192\n"
"buffer_load_dwordx4 a[160:163], v13, s[8:11], s6 offen offset:0 sc1\n"
"s_waitcnt lgkmcnt(4)\n"
"v_mfma_f32_32x32x16_bf16 v[94:109], v[30:33], a[128:131], 0\n"
"ds_read_b64_tr_b16 v[70:71], v15 offset:4096\n"
"ds_read_b64_tr_b16 v[72:73], v15 offset:5120\n"
"v_mfma_f32_32x32x16_bf16 v[110:125], v[30:33], a[132:135], 0\n"
"buffer_load_dwordx4 a[164:167], v14, s[8:11], s6 offen offset:0 sc1\n"
"s_waitcnt vmcnt(8) lgkmcnt(4)\n"
"v_mfma_f32_32x32x16_bf16 v[54:69], v[42:45], a[136:139], v[54:69]\n"
"ds_read_b64_tr_b16 v[126:127], v16 offset:4096\n"
"ds_read_b64_tr_b16 v[128:129], v16 offset:5120\n"
"s_waitcnt vmcnt(7)\n"
"v_mfma_f32_32x32x16_bf16 v[78:93], v[42:45], a[140:143], v[78:93]\n"
"buffer_load_dwordx4 a[168:171], v13, s[8:11], s6 offen offset:1024 sc1\n"
"s_waitcnt lgkmcnt(4)\n"
"v_mfma_f32_32x32x16_bf16 v[94:109], v[46:49], a[136:139], v[94:109]\n"
"ds_read_b64_tr_b16 v[26:27], v15 offset:6144\n"
"ds_read_b64_tr_b16 v[28:29], v15 offset:7168\n"
"v_mfma_f32_32x32x16_bf16 v[110:125], v[46:49], a[140:143], v[110:125]\n"
"buffer_load_dwordx4 a[172:175], v14, s[8:11], s6 offen offset:1024 sc1\n"
"s_waitcnt vmcnt(8) lgkmcnt(4)\n"
"v_mfma_f32_32x32x16_bf16 v[54:69], v[70:73], a[144:147], v[54:69]\n"
"ds_read_b64_tr_b16 v[30:31], v16 offset:6144\n"
"ds_read_b64_tr_b16 v[32:33], v16 offset:7168\n"
"s_waitcnt vmcnt(7)\n"
"v_mfma_f32_32x32x16_bf16 v[78:93], v[70:73], a[148:151], v[78:93]\n"
"buffer_load_dwordx4 a[176:179], v13, s[8:11], s6 offen offset:2048 sc1\n"
"s_waitcnt lgkmcnt(4)\n"
"v_mfma_f32_32x32x16_bf16 v[94:109], v[126:129], a[144:147], v[94:109]\n"
"ds_read_b64_tr_b16 v[42:43], v15 offset:8192\n"
"ds_read_b64_tr_b16 v[44:45], v15 offset:9216\n"
"v_mfma_f32_32x32x16_bf16 v[110:125], v[126:129], a[148:151], v[110:125]\n"
"buffer_load_dwordx4 a[180:183], v14, s[8:11], s6 offen offset:2048 sc1\n"
"s_waitcnt vmcnt(8) lgkmcnt(4)\n"
"v_mfma_f32_32x32x16_bf16 v[54:69], v[26:29], a[152:155], v[54:69]\n"
"ds_read_b64_tr_b16 v[46:47], v16 offset:8192\n"
"ds_read_b64_tr_b16 v[48:49], v16 offset:9216\n"
"s_waitcnt vmcnt(7)\n"
"v_mfma_f32_32x32x16_bf16 v[78:93], v[26:29], a[156:159], v[78:93]\n"
"buffer_load_dwordx4 a[184:187], v13, s[8:11], s6 offen offset:3072 sc1\n"
"s_waitcnt lgkmcnt(4)\n"
"v_mfma_f32_32x32x16_bf16 v[94:109], v[30:33], a[152:155], v[94:109]\n"
"ds_read_b64_tr_b16 v[70:71], v15 offset:10240\n"
"ds_read_b64_tr_b16 v[72:73], v15 offset:11264\n"
"v_mfma_f32_32x32x16_bf16 v[110:125], v[30:33], a[156:159], v[110:125]\n"
"buffer_load_dwordx4 a[188:191], v14, s[8:11], s6 offen offset:3072 sc1\n"
"s_waitcnt vmcnt(7) lgkmcnt(4)\n"
"v_mfma_f32_32x32x16_bf16 v[54:69], v[42:45], a[160:163], v[54:69]\n"
"ds_read_b64_tr_b16 v[126:127], v16 offset:10240\n"
"ds_read_b64_tr_b16 v[128:129], v16 offset:11264\n"
"s_waitcnt vmcnt(6)\n"
"v_mfma_f32_32x32x16_bf16 v[78:93], v[42:45], a[164:167], v[78:93]\n"
"s_mov_b32 s6, 16384\n"
"buffer_load_dwordx4 a[192:195], v13, s[8:11], s6 offen offset:0 sc1\n"
"s_waitcnt lgkmcnt(4)\n"
"v_mfma_f32_32x32x16_bf16 v[94:109], v[46:49], a[160:163], v[94:109]\n"
"ds_read_b64_tr_b16 v[26:27], v17 offset:0\n"
"ds_read_b64_tr_b16 v[28:29], v17 offset:1024\n"
"v_mfma_f32_32x32x16_bf16 v[110:125], v[46:49], a[164:167], v[110:125]\n"
"buffer_load_dwordx4 a[196:199], v14, s[8:11], s6 offen offset:0 sc1\n"
"s_waitcnt vmcnt(7) lgkmcnt(4)\n"
"v_mfma_f32_32x32x16_bf16 v[54:69], v[70:73], a[168:171], v[54:69]\n"
"ds_read_b64_tr_b16 v[30:31], v20 offset:0\n"
"ds_read_b64_tr_b16 v[32:33], v20 offset:1024\n"
"s_waitcnt vmcnt(6)\n"
"v_mfma_f32_32x32x16_bf16 v[78:93], v[70:73], a[172:175], v[78:93]\n"
"buffer_load_dwordx4 a[200:203], v13, s[8:11], s6 offen offset:1024 sc1\n"
"s_waitcnt lgkmcnt(4)\n"
"v_mfma_f32_32x32x16_bf16 v[94:109], v[126:129], a[168:171], v[94:109]\n"
"ds_read_b64_tr_b16 v[42:43], v17 offset:2048\n"
"ds_read_b64_tr_b16 v[44:45], v17 offset:3072\n"
"v_mfma_f32_32x32x16_bf16 v[110:125], v[126:129], a[172:175], v[110:125]\n"
"buffer_load_dwordx4 a[204:207], v14, s[8:11], s6 offen offset:1024 sc1\n"
"s_waitcnt vmcnt(7) lgkmcnt(4)\n"
"v_mfma_f32_32x32x16_bf16 v[54:69], v[26:29], a[176:179], v[54:69]\n"
"ds_read_b64_tr_b16 v[46:47], v20 offset:2048\n"
"ds_read_b64_tr_b16 v[48:49], v20 offset:3072\n"
"s_waitcnt vmcnt(6)\n"
"v_mfma_f32_32x32x16_bf16 v[78:93], v[26:29], a[180:183], v[78:93]\n"
"buffer_load_dwordx4 a[208:211], v13, s[8:11], s6 offen offset:2048 sc1\n"
"s_waitcnt lgkmcnt(4)\n"
"v_mfma_f32_32x32x16_bf16 v[94:109], v[30:33], a[176:179], v[94:109]\n"
"ds_read_b64_tr_b16 v[70:71], v17 offset:4096\n"
"ds_read_b64_tr_b16 v[72:73], v17 offset:5120\n"
"v_mfma_f32_32x32x16_bf16 v[110:125], v[30:33], a[180:183], v[110:125]\n"
"buffer_load_dwordx4 a[212:215], v14, s[8:11], s6 offen offset:2048 sc1\n"
"s_waitcnt vmcnt(7) lgkmcnt(4)\n"
"v_mfma_f32_32x32x16_bf16 v[54:69], v[42:45], a[184:187], v[54:69]\n"
"ds_read_b64_tr_b16 v[126:127], v20 offset:4096\n"
"ds_read_b64_tr_b16 v[128:129], v20 offset:5120\n"
"s_waitcnt vmcnt(6)\n"
"v_mfma_f32_32x32x16_bf16 v[78:93], v[42:45], a[188:191], v[78:93]\n"
"buffer_load_dwordx4 a[216:219], v13, s[8:11], s6 offen offset:3072 sc1\n"
"s_waitcnt lgkmcnt(4)\n"
"v_mfma_f32_32x32x16_bf16 v[94:109], v[46:49], a[184:187], v[94:109]\n"
"ds_read_b64_tr_b16 v[26:27], v21 offset:0\n"
"ds_read_b64_tr_b16 v[28:29], v21 offset:1024\n"
"v_mfma_f32_32x32x16_bf16 v[110:125], v[46:49], a[188:191], v[110:125]\n"
"buffer_load_dwordx4 a[220:223], v14, s[8:11], s6 offen offset:3072 sc1\n"
"s_waitcnt vmcnt(7) lgkmcnt(4)\n"
"v_mfma_f32_32x32x16_bf16 v[54:69], v[70:73], a[192:195], v[54:69]\n"
"ds_read_b64_tr_b16 v[30:31], v22 offset:0\n"
"ds_read_b64_tr_b16 v[32:33], v22 offset:1024\n"
"s_waitcnt vmcnt(6)\n"
"v_mfma_f32_32x32x16_bf16 v[78:93], v[70:73], a[196:199], v[78:93]\n"
"s_mov_b32 s6, 24576\n"
"buffer_load_dwordx4 a[224:227], v13, s[8:11], s6 offen offset:0 sc1\n"
"s_waitcnt lgkmcnt(4)\n"
"v_mfma_f32_32x32x16_bf16 v[94:109], v[126:129], a[192:195], v[94:109]\n"
"ds_read_b64_tr_b16 v[42:43], v21 offset:2048\n"
"ds_read_b64_tr_b16 v[44:45], v21 offset:3072\n"
"v_mfma_f32_32x32x16_bf16 v[110:125], v[126:129], a[196:199], v[110:125]\n"
"buffer_load_dwordx4 a[228:231], v14, s[8:11], s6 offen offset:0 sc1\n"
"s_waitcnt vmcnt(7) lgkmcnt(4)\n"
"v_mfma_f32_32x32x16_bf16 v[54:69], v[26:29], a[200:203], v[54:69]\n"
"ds_read_b64_tr_b16 v[46:47], v22 offset:2048\n"
"ds_read_b64_tr_b16 v[48:49], v22 offset:3072\n"
"s_waitcnt vmcnt(6)\n"
"v_mfma_f32_32x32x16_bf16 v[78:93], v[26:29], a[204:207], v[78:93]\n"
"buffer_load_dwordx4 a[232:235], v13, s[8:11], s6 offen offset:1024 sc1\n"
"s_waitcnt lgkmcnt(4)\n"
"v_mfma_f32_32x32x16_bf16 v[94:109], v[30:33], a[200:203], v[94:109]\n"
"ds_read_b64_tr_b16 v[70:71], v21 offset:4096\n"
"ds_read_b64_tr_b16 v[72:73], v21 offset:5120\n"
"v_mfma_f32_32x32x16_bf16 v[110:125], v[30:33], a[204:207], v[110:125]\n"
"buffer_load_dwordx4 a[236:239], v14, s[8:11], s6 offen offset:1024 sc1\n"
"s_waitcnt vmcnt(7) lgkmcnt(4)\n"
"v_mfma_f32_32x32x16_bf16 v[54:69], v[42:45], a[208:211], v[54:69]\n"
"ds_read_b64_tr_b16 v[126:127], v22 offset:4096\n"
"ds_read_b64_tr_b16 v[128:129], v22 offset:5120\n"
"s_waitcnt vmcnt(6)\n"
"v_mfma_f32_32x32x16_bf16 v[78:93], v[42:45], a[212:215], v[78:93]\n"
"buffer_load_dwordx4 a[240:243], v13, s[8:11], s6 offen offset:2048 sc1\n"
"s_waitcnt lgkmcnt(4)\n"
"v_mfma_f32_32x32x16_bf16 v[94:109], v[46:49], a[208:211], v[94:109]\n"
"ds_read_b64_tr_b16 v[26:27], v21 offset:12288\n"
"ds_read_b64_tr_b16 v[28:29], v21 offset:13312\n"
"v_mfma_f32_32x32x16_bf16 v[110:125], v[46:49], a[212:215], v[110:125]\n"
"buffer_load_dwordx4 a[244:247], v14, s[8:11], s6 offen offset:2048 sc1\n"
"s_waitcnt vmcnt(7) lgkmcnt(4)\n"
"v_mfma_f32_32x32x16_bf16 v[54:69], v[70:73], a[216:219], v[54:69]\n"
"ds_read_b64_tr_b16 v[30:31], v22 offset:12288\n"
"ds_read_b64_tr_b16 v[32:33], v22 offset:13312\n"
"s_waitcnt vmcnt(6)\n"
"v_mfma_f32_32x32x16_bf16 v[78:93], v[70:73], a[220:223], v[78:93]\n"
"buffer_load_dwordx4 a[248:251], v13, s[8:11], s6 offen offset:3072 sc1\n"
"s_waitcnt lgkmcnt(4)\n"
"v_mfma_f32_32x32x16_bf16 v[94:109], v[126:129], a[216:219], v[94:109]\n"
"ds_read_b64_tr_b16 v[42:43], v21 offset:14336\n"
"ds_read_b64_tr_b16 v[44:45], v21 offset:15360\n"
"v_mfma_f32_32x32x16_bf16 v[110:125], v[126:129], a[220:223], v[110:125]\n"
"buffer_load_dwordx4 a[252:255], v14, s[8:11], s6 offen offset:3072 sc1\n"
"s_waitcnt vmcnt(7) lgkmcnt(4)\n"
"v_mfma_f32_32x32x16_bf16 v[54:69], v[26:29], a[224:227], v[54:69]\n"
"ds_read_b64_tr_b16 v[46:47], v22 offset:14336\n"
"ds_read_b64_tr_b16 v[48:49], v22 offset:15360\n"
"s_waitcnt vmcnt(6)\n"
"v_mfma_f32_32x32x16_bf16 v[78:93], v[26:29], a[228:231], v[78:93]\n"
"buffer_load_dwordx2 v[34:35], v18, s[8:11], s26 offen sc1\n"
"s_waitcnt lgkmcnt(4)\n"
"v_mfma_f32_32x32x16_bf16 v[94:109], v[30:33], a[224:227], v[94:109]\n"
"ds_read_b64_tr_b16 v[70:71], v21 offset:16384\n"
"ds_read_b64_tr_b16 v[72:73], v21 offset:17408\n"
"v_mfma_f32_32x32x16_bf16 v[110:125], v[30:33], a[228:231], v[110:125]\n"
"s_waitcnt vmcnt(6) lgkmcnt(4)\n"
"v_mfma_f32_32x32x16_bf16 v[54:69], v[42:45], a[232:235], v[54:69]\n"
"ds_read_b64_tr_b16 v[126:127], v22 offset:16384\n"
"ds_read_b64_tr_b16 v[128:129], v22 offset:17408\n"
"s_waitcnt vmcnt(5)\n"
"v_mfma_f32_32x32x16_bf16 v[78:93], v[42:45], a[236:239], v[78:93]\n"
"s_waitcnt lgkmcnt(4)\n"
"v_mfma_f32_32x32x16_bf16 v[94:109], v[46:49], a[232:235], v[94:109]\n"
"ds_read_b64_tr_b16 v[26:27], v21 offset:24576\n"
"ds_read_b64_tr_b16 v[28:29], v21 offset:25600\n"
"v_mfma_f32_32x32x16_bf16 v[110:125], v[46:49], a[236:239], v[110:125]\n"
"s_waitcnt vmcnt(0)\n"
"v_cmp_ne_u64 vcc, s[12:13], v[34:35]\n"
"s_nop 4\n"
"s_cbranch_vccnz .Lretry6_%=\n"
".Lback6_%=:\n"
"s_mov_b32 s6, 4096\n"
"buffer_load_dwordx4 a[0:3], v13, s[8:11], s6 offen offset:0 sc1\n"
"s_add_u32 m0, s66, 0\n"
"s_add_u32 s3, s65, 98304\n"
"buffer_load_dwordx4 v39, s[56:59], s3 offen lds\n"
"buffer_load_dwordx4 a[4:7], v14, s[8:11], s6 offen offset:0 sc1\n"
"buffer_load_dwordx4 v40, s[56:59], s3 offen offset:1024 lds\n"
"s_waitcnt lgkmcnt(4)\n"
"v_mfma_f32_32x32x16_bf16 v[54:69], v[70:73], a[240:243], v[54:69]\n"
"ds_read_b64_tr_b16 v[30:31], v22 offset:24576\n"
"ds_read_b64_tr_b16 v[32:33], v22 offset:25600\n"
"v_mfma_f32_32x32x16_bf16 v[78:93], v[70:73], a[244:247], v[78:93]\n"
"buffer_load_dwordx4 a[8:11], v13, s[8:11], s6 offen offset:1024 sc1\n"
"s_add_u32 m0, s66, 2048\n"
"s_add_u32 s3, s65, 114688\n"
"buffer_load_dwordx4 v39, s[56:59], s3 offen lds\n"
"s_waitcnt lgkmcnt(4)\n"
"v_mfma_f32_32x32x16_bf16 v[94:109], v[126:129], a[240:243], v[94:109]\n"
"ds_read_b64_tr_b16 v[42:43], v21 offset:26624\n"
"ds_read_b64_tr_b16 v[44:45], v21 offset:27648\n"
"v_mfma_f32_32x32x16_bf16 v[110:125], v[126:129], a[244:247], v[110:125]\n"
"buffer_load_dwordx4 a[12:15], v14, s[8:11], s6 offen offset:1024 sc1\n"
"buffer_load_dwordx4 v40, s[56:59], s3 offen offset:1024 lds\n"
"s_waitcnt lgkmcnt(4)\n"
"v_mfma_f32_32x32x16_bf16 v[54:69], v[26:29], a[248:251], v[54:69]\n"
"ds_read_b64_tr_b16 v[46:47], v22 offset:26624\n"
"ds_read_b64_tr_b16 v[48:49], v22 offset:27648\n"
"v_mfma_f32_32x32x16_bf16 v[78:93], v[26:29], a[252:255], v[78:93]\n"
"buffer_load_dwordx4 a[16:19], v13, s[8:11], s6 offen offset:2048 sc1\n"
"s_add_u32 m0, s66, 4096\n"
"s_add_u32 s3, s65, 196608\n"
"buffer_load_dwordx4 v39, s[56:59], s3 offen lds\n"
"s_waitcnt lgkmcnt(4)\n"
"v_mfma_f32_32x32x16_bf16 v[94:109], v[30:33], a[248:251], v[94:109]\n"
"ds_read_b64_tr_b16 v[70:71], v21 offset:28672\n"
"ds_read_b64_tr_b16 v[72:73], v21 offset:29696\n"
"v_mfma_f32_32x32x16_bf16 v[110:125], v[30:33], a[252:255], v[110:125]\n"
"buffer_load_dwordx4 a[20:23], v14, s[8:11], s6 offen offset:2048 sc1\n"
"buffer_load_dwordx4 v40, s[56:59], s3 offen offset:1024 lds\n"
"s_waitcnt vmcnt(11) lgkmcnt(4)\n"
"v_mfma_f32_32x32x16_bf16 v[54:69], v[42:45], a[0:3], v[54:69]\n"
"ds_read_b64_tr_b16 v[126:127], v22 offset:28672\n"
"ds_read_b64_tr_b16 v[128:129], v22 offset:29696\n"
"s_waitcnt vmcnt(9)\n"
"v_mfma_f32_32x32x16_bf16 v[78:93], v[42:45], a[4:7], v[78:93]\n"
"buffer_load_dwordx4 a[24:27], v13, s[8:11], s6 offen offset:3072 sc1\n"
"s_add_u32 m0, s66, 6144\n"
"s_add_u32 s3, s65, 212992\n"
"buffer_load_dwordx4 v39, s[56:59], s3 offen lds\n"
"s_waitcnt lgkmcnt(4)\n"
"v_mfma_f32_32x32x16_bf16 v[94:109], v[46:49], a[0:3], v[94:109]\n"
"s_waitcnt vmcnt(10)\n"
"ds_read_b64_tr_b16 v[26:27], v15 offset:0\n"
"ds_read_b64_tr_b16 v[28:29], v15 offset:1024\n"
"v_mfma_f32_32x32x16_bf16 v[110:125], v[46:49], a[4:7], v[110:125]\n"
"buffer_load_dwordx4 a[28:31], v14, s[8:11], s6 offen offset:3072 sc1\n"
"buffer_load_dwordx4 v40, s[56:59], s3 offen offset:1024 lds\n"
"s_waitcnt vmcnt(11) lgkmcnt(4)\n"
"v_mfma_f32_32x32x16_bf16 v[54:69], v[70:73], a[8:11], v[54:69]\n"
"ds_read_b64_tr_b16 v[30:31], v16 offset:0\n"
"ds_read_b64_tr_b16 v[32:33], v16 offset:1024\n"
"s_waitcnt vmcnt(9)\n"
"v_mfma_f32_32x32x16_bf16 v[78:93], v[70:73], a[12:15], v[78:93]\n"
"s_mov_b32 s6, 12288\n"
"buffer_load_dwordx4 a[32:35], v13, s[8:11], s6 offen offset:0 sc1\n"
"s_add_u32 m0, s66, 8192\n"
"s_add_u32 s3, s65, 229376\n"
"buffer_load_dwordx4 v39, s[56:59], s3 offen lds\n"
"s_waitcnt lgkmcnt(4)\n"
"v_mfma_f32_32x32x16_bf16 v[94:109], v[126:129], a[8:11], v[94:109]\n"
"s_waitcnt vmcnt(10)\n"
"ds_read_b64_tr_b16 v[42:43], v15 offset:2048\n"
"ds_read_b64_tr_b16 v[44:45], v15 offset:3072\n"
"v_mfma_f32_32x32x16_bf16 v[110:125], v[126:129], a[12:15], v[110:125]\n"
"buffer_load_dwordx4 a[36:39], v14, s[8:11], s6 offen offset:0 sc1\n"
"buffer_load_dwordx4 v40, s[56:59], s3 offen offset:1024 lds\n"
"s_waitcnt vmcnt(11) lgkmcnt(4)\n"
"v_mfma_f32_32x32x16_bf16 v[54:69], v[26:29], a[16:19], v[54:69]\n"
"ds_read_b64_tr_b16 v[46:47], v16 offset:2048\n"
"ds_read_b64_tr_b16 v[48:49], v16 offset:3072\n"
"s_waitcnt vmcnt(9)\n"
"v_mfma_f32_32x32x16_bf16 v[78:93], v[26:29], a[20:23], v[78:93]\n"
"buffer_load_dwordx4 a[40:43], v13, s[8:11], s6 offen offset:1024 sc1\n"
"s_add_u32 m0, s66, 10240\n"
"s_add_u32 s3, s65, 245760\n"
"buffer_load_dwordx4 v39, s[56:59], s3 offen lds\n"
"s_waitcnt lgkmcnt(4)\n"
"v_mfma_f32_32x32x16_bf16 v[94:109], v[30:33], a[16:19], v[94:109]\n"
"s_waitcnt vmcnt(10)\n"
"ds_read_b64_tr_b16 v[70:71], v15 offset:4096\n"
"ds_read_b64_tr_b16 v[72:73], v15 offset:5120\n"
"v_mfma_f32_32x32x16_bf16 v[110:125], v[30:33], a[20:23], v[110:125]\n"
"buffer_load_dwordx4 a[44:47], v14, s[8:11], s6 offen offset:1024 sc1\n"
"buffer_load_dwordx4 v40, s[56:59], s3 offen offset:1024 lds\n"
"s_waitcnt vmcnt(11) lgkmcnt(4)\n"
"v_mfma_f32_32x32x16_bf16 v[54:69], v[42:45], a[24:27], v[54:69]\n"
"ds_read_b64_tr_b16 v[126:127], v16 offset:4096\n"
"ds_read_b64_tr_b16 v[128:129], v16 offset:5120\n"
"s_waitcnt vmcnt(9)\n"
"v_mfma_f32_32x32x16_bf16 v[78:93], v[42:45], a[28:31], v[78:93]\n"
"buffer_load_dwordx4 a[48:51], v13, s[8:11], s6 offen offset:2048 sc1\n"
"s_add_u32 m0, s67, 0\n"
"s_add_u32 s3, s65, 327680\n"
"buffer_load_dwordx4 v39, s[56:59], s3 offen lds\n"
"s_waitcnt lgkmcnt(4)\n"
"v_mfma_f32_32x32x16_bf16 v[94:109], v[46:49], a[24:27], v[94:109]\n"
"s_waitcnt vmcnt(10)\n"
"ds_read_b64_tr_b16 v[26:27], v15 offset:6144\n"
"ds_read_b64_tr_b16 v[28:29], v15 offset:7168\n"
"v_mfma_f32_32x32x16_bf16 v[110:125], v[46:49], a[28:31], v[110:125]\n"
"buffer_load_dwordx4 a[52:55], v14, s[8:11], s6 offen offset:2048 sc1\n"
"buffer_load_dwordx4 v40, s[56:59], s3 offen offset:1024 lds\n"
"s_waitcnt vmcnt(11) lgkmcnt(4)\n"
"v_mfma_f32_32x32x16_bf16 v[54:69], v[70:73], a[32:35], v[54:69]\n"
"ds_read_b64_tr_b16 v[30:31], v16 offset:6144\n"
"ds_read_b64_tr_b16 v[32:33], v16 offset:7168\n"
"s_waitcnt vmcnt(9)\n"
"v_mfma_f32_32x32x16_bf16 v[78:93], v[70:73], a[36:39], v[78:93]\n"
"buffer_load_dwordx4 a[56:59], v13, s[8:11], s6 offen offset:3072 sc1\n"
"s_add_u32 m0, s67, 2048\n"
"s_add_u32 s3, s65, 344064\n"
"buffer_load_dwordx4 v39, s[56:59], s3 offen lds\n"
"s_waitcnt lgkmcnt(4)\n"
"v_mfma_f32_32x32x16_bf16 v[94:109], v[126:129], a[32:35], v[94:109]\n"
"s_waitcnt vmcnt(10)\n"
"ds_read_b64_tr_b16 v[42:43], v15 offset:8192\n"
"ds_read_b64_tr_b16 v[44:45], v15 offset:9216\n"
"v_mfma_f32_32x32x16_bf16 v[110:125], v[126:129], a[36:39], v[110:125]\n"
"buffer_load_dwordx4 a[60:63], v14, s[8:11], s6 offen offset:3072 sc1\n"
"buffer_load_dwordx4 v40, s[56:59], s3 offen offset:1024 lds\n"
"s_waitcnt vmcnt(11) lgkmcnt(4)\n"
"v_mfma_f32_32x32x16_bf16 v[54:69], v[26:29], a[40:43], v[54:69]\n"
"ds_read_b64_tr_b16 v[46:47], v16 offset:8192\n"
"ds_read_b64_tr_b16 v[48:49], v16 offset:9216\n"
"s_waitcnt vmcnt(9)\n"
"v_mfma_f32_32x32x16_bf16 v[78:93], v[26:29], a[44:47], v[78:93]\n"
"s_mov_b32 s6, 20480\n"
"buffer_load_dwordx4 a[64:67], v13, s[8:11], s6 offen offset:0 sc1\n"
"s_add_u32 m0, s67, 4096\n"
"s_add_u32 s3, s65, 360448\n"
"buffer_load_dwordx4 v39, s[56:59], s3 offen lds\n"
"s_waitcnt lgkmcnt(4)\n"
"v_mfma_f32_32x32x16_bf16 v[94:109], v[30:33], a[40:43], v[94:109]\n"
"s_waitcnt vmcnt(10)\n"
"ds_read_b64_tr_b16 v[70:71], v15 offset:10240\n"
"ds_read_b64_tr_b16 v[72:73], v15 offset:11264\n"
"v_mfma_f32_32x32x16_bf16 v[110:125], v[30:33], a[44:47], v[110:125]\n"
"buffer_load_dwordx4 a[68:71], v14, s[8:11], s6 offen offset:0 sc1\n"
"buffer_load_dwordx4 v40, s[56:59], s3 offen offset:1024 lds\n"
"s_waitcnt vmcnt(11) lgkmcnt(4)\n"
"v_mfma_f32_32x32x16_bf16 v[54:69], v[42:45], a[48:51], v[54:69]\n"
"ds_read_b64_tr_b16 v[126:127], v16 offset:10240\n"
"ds_read_b64_tr_b16 v[128:129], v16 offset:11264\n"
"s_waitcnt vmcnt(9)\n"
"v_mfma_f32_32x32x16_bf16 v[78:93], v[42:45], a[52:55], v[78:93]\n"
"buffer_load_dwordx4 a[72:75], v13, s[8:11], s6 offen offset:1024 sc1\n"
"s_add_u32 m0, s68, 0\n"
"s_add_u32 s3, s65, 376832\n"
"buffer_load_dwordx4 v39, s[56:59], s3 offen lds\n"
"s_waitcnt lgkmcnt(4)\n"
"v_mfma_f32_32x32x16_bf16 v[94:109], v[46:49], a[48:51], v[94:109]\n"
"s_waitcnt vmcnt(10)\n"
"ds_read_b64_tr_b16 v[26:27], v17 offset:0\n"
"ds_read_b64_tr_b16 v[28:29], v17 offset:1024\n"
"v_mfma_f32_32x32x16_bf16 v[110:125], v[46:49], a[52:55], v[110:125]\n"
"buffer_load_dwordx4 a[76:79], v14, s[8:11], s6 offen offset:1024 sc1\n"
"buffer_load_dwordx4 v40, s[56:59], s3 offen offset:1024 lds\n"
"s_waitcnt vmcnt(11) lgkmcnt(4)\n"
"v_mfma_f32_32x32x16_bf16 v[54:69], v[70:73], a[56:59], v[54:69]\n"
"ds_read_b64_tr_b16 v[30:31], v20 offset:0\n"
"ds_read_b64_tr_b16 v[32:33], v20 offset:1024\n"
"s_waitcnt vmcnt(9)\n"
"v_mfma_f32_32x32x16_bf16 v[78:93], v[70:73], a[60:63], v[78:93]\n"
"buffer_load_dwordx4 a[80:83], v13, s[8:11], s6 offen offset:2048 sc1\n"
"buffer_load_dwordx4 a[84:87], v14, s[8:11], s6 offen offset:2048 sc1\n"
"s_waitcnt lgkmcnt(4)\n"
"v_mfma_f32_32x32x16_bf16 v[94:109], v[126:129], a[56:59], v[94:109]\n"
"s_waitcnt vmcnt(10)\n"
"ds_read_b64_tr_b16 v[42:43], v17 offset:2048\n"
"ds_read_b64_tr_b16 v[44:45], v17 offset:3072\n"
"v_mfma_f32_32x32x16_bf16 v[110:125], v[126:129], a[60:63], v[110:125]\n"
"buffer_load_dwordx4 a[88:91], v13, s[8:11], s6 offen offset:3072 sc1\n"
"buffer_load_dwordx4 a[92:95], v14, s[8:11], s6 offen offset:3072 sc1\n"
"s_waitcnt vmcnt(11) lgkmcnt(4)\n"
"v_mfma_f32_32x32x16_bf16 v[54:69], v[26:29], a[64:67], v[54:69]\n"
"ds_read_b64_tr_b16 v[46:47], v20 offset:2048\n"
"ds_read_b64_tr_b16 v[48:49], v20 offset:3072\n"
"s_waitcnt vmcnt(9)\n"
"v_mfma_f32_32x32x16_bf16 v[78:93], v[26:29], a[68:71], v[78:93]\n"
"buffer_load_dwordx4 v[130:133], v4, s[8:11], s20 offen offset:0 sc1\n"
"buffer_load_dwordx4 v[134:137], v4, s[8:11], s20 offen offset:1024 sc1\n"
"s_waitcnt lgkmcnt(4)\n"
"v_mfma_f32_32x32x16_bf16 v[94:109], v[30:33], a[64:67], v[94:109]\n"
"s_waitcnt vmcnt(10)\n"
"ds_read_b64_tr_b16 v[70:71], v17 offset:4096\n"
"ds_read_b64_tr_b16 v[72:73], v17 offset:5120\n"
"v_mfma_f32_32x32x16_bf16 v[110:125], v[30:33], a[68:71], v[110:125]\n"
"buffer_load_dwordx2 v[138:139], v23, s[8:11], s21 offen offset:0 sc1\n"
"buffer_load_dwordx2 v[140:141], v23, s[8:11], s21 offen offset:256 sc1\n"
"s_waitcnt vmcnt(11) lgkmcnt(4)\n"
"v_mfma_f32_32x32x16_bf16 v[54:69], v[42:45], a[72:75], v[54:69]\n"
"ds_read_b64_tr_b16 v[126:127], v20 offset:4096\n"
"ds_read_b64_tr_b16 v[128:129], v20 offset:5120\n"
"s_waitcnt vmcnt(9)\n"
"v_mfma_f32_32x32x16_bf16 v[78:93], v[42:45], a[76:79], v[78:93]\n"
"buffer_load_dwordx2 v[142:143], v23, s[8:11], s21 offen offset:512 sc1\n"
"buffer_load_dwordx2 v[144:145], v23, s[8:11], s21 offen offset:768 sc1\n"
"s_waitcnt lgkmcnt(4)\n"
"v_mfma_f32_32x32x16_bf16 v[94:109], v[46:49], a[72:75], v[94:109]\n"
"s_waitcnt vmcnt(10)\n"
"ds_read_b64_tr_b16 v[26:27], v21 offset:0\n"
"ds_read_b64_tr_b16 v[28:29], v21 offset:1024\n"
"v_mfma_f32_32x32x16_bf16 v[110:125], v[46:49], a[76:79], v[110:125]\n"
"buffer_load_dwordx2 v[146:147], v23, s[8:11], s21 offen offset:1024 sc1\n"
"buffer_load_dwordx2 v[148:149], v23, s[8:11], s21 offen offset:1280 sc1\n"
"s_waitcnt vmcnt(11) lgkmcnt(4)\n"
"v_mfma_f32_32x32x16_bf16 v[54:69], v[70:73], a[80:83], v[54:69]\n"
"ds_read_b64_tr_b16 v[30:31], v22 offset:0\n"
"ds_read_b64_tr_b16 v[32:33], v22 offset:1024\n"
"s_waitcnt vmcnt(10)\n"
"v_mfma_f32_32x32x16_bf16 v[78:93], v[70:73], a[84:87], v[78:93]\n"
"buffer_load_dwordx2 v[150:151], v23, s[8:11], s21 offen offset:1536 sc1\n"
"buffer_load_dwordx2 v[152:153], v23, s[8:11], s21 offen offset:1792 sc1\n"
"s_waitcnt lgkmcnt(4)\n"
"v_mfma_f32_32x32x16_bf16 v[94:109], v[126:129], a[80:83], v[94:109]\n"
"v_mfma_f32_32x32x16_bf16 v[110:125], v[126:129], a[84:87], v[110:125]\n"
"s_waitcnt vmcnt(11) lgkmcnt(2)\n"
"v_mfma_f32_32x32x16_bf16 v[54:69], v[26:29], a[88:91], v[54:69]\n"
"s_waitcnt vmcnt(10)\n"
"v_mfma_f32_32x32x16_bf16 v[78:93], v[26:29], a[92:95], v[78:93]\n"
"s_waitcnt lgkmcnt(0)\n"
"v_mfma_f32_32x32x16_bf16 v[94:109], v[30:33], a[88:91], v[94:109]\n"
"v_mfma_f32_32x32x16_bf16 v[110:125], v[30:33], a[92:95], v[110:125]\n"
"v_add_u32 v36, s68, v4\n"
"s_nop 7\n"
"ds_write_b128 v36, v[78:81] offset:2048\n"
"ds_write_b128 v36, v[82:85] offset:3072\n"
"ds_write_b128 v36, v[86:89] offset:4096\n"
"ds_write_b128 v36, v[90:93] offset:5120\n"
"ds_write_b128 v36, v[94:97] offset:12288\n"
"ds_write_b128 v36, v[98:101] offset:13312\n"
"ds_write_b128 v36, v[102:105] offset:14336\n"
"ds_write_b128 v36, v[106:109] offset:15360\n"
"ds_write_b128 v36, v[110:113] offset:24576\n"
"ds_write_b128 v36, v[114:117] offset:25600\n"
"ds_write_b128 v36, v[118:121] offset:26624\n"
"ds_write_b128 v36, v[122:125] offset:27648\n"
"s_xor_b32 s40, s15, 1\n"
"s_lshr_b32 s41, s40, 1\n"
"s_and_b32 s40, s40, 1\n"
"s_mul_i32 s41, s41, 36992\n"
"s_mul_i32 s40, s40, 6144\n"
"s_add_u32 s40, s40, s41\n"
"v_add_u32 v37, s40, v4\n"
"s_xor_b32 s40, s15, 2\n"
"s_lshr_b32 s41, s40, 1\n"
"s_and_b32 s40, s40, 1\n"
"s_mul_i32 s41, s41, 36992\n"
"s_mul_i32 s40, s40, 6144\n"
"s_add_u32 s40, s40, s41\n"
"v_add_u32 v38, s40, v4\n"
"s_xor_b32 s40, s15, 3\n"
"s_lshr_b32 s41, s40, 1\n"
"s_and_b32 s40, s40, 1\n"
"s_mul_i32 s41, s41, 36992\n"
"s_mul_i32 s40, s40, 6144\n"
"s_add_u32 s40, s40, s41\n"
"v_add_u32 v41, s40, v4\n"
"s_waitcnt vmcnt(3)\n"
"v_pk_add_f32 v[138:139], v[138:139], v[146:147]\n"
"s_waitcnt vmcnt(2)\n"
"v_pk_add_f32 v[140:141], v[140:141], v[148:149]\n"
"s_waitcnt vmcnt(1)\n"
"v_pk_add_f32 v[142:143], v[142:143], v[150:151]\n"
"s_waitcnt vmcnt(0)\n"
"v_pk_add_f32 v[144:145], v[144:145], v[152:153]\n"
"v_pk_add_f32 v[138:139], v[138:139], v[142:143]\n"
"v_pk_add_f32 v[140:141], v[140:141], v[144:145]\n"
"v_pk_add_f32 v[138:139], v[138:139], v[140:141]\n"
"v_mov_b32 v5, v138\n"
"v_mov_b32 v6, v139\n"
"s_nop 0\n"
"v_permlane32_swap_b32 v138, v5\n"
"v_permlane32_swap_b32 v139, v6\n"
"v_add_f32 v138, v138, v5\n"
"v_add_f32 v139, v139, v6\n"
"v_mul_f32 v7, 989855744, v138\n"
"v_mul_f32 v5, v7, v7\n"
"v_mov_b32 v6, 989855744\n"
"v_fma_f32 v5, v139, v6, -v5\n"
"v_max_f32 v5, 0, v5\n"
"v_add_f32 v5, 925353388, v5\n"
"v_rcp_f32 v138, v5\n"
"s_waitcnt lgkmcnt(0)\n"
"v_lshlrev_b32 v110, 16, v130\n"
"v_and_b32 v111, 0xffff0000, v130\n"
"v_lshlrev_b32 v112, 16, v131\n"
"v_and_b32 v113, 0xffff0000, v131\n"
"v_lshlrev_b32 v114, 16, v132\n"
"v_and_b32 v115, 0xffff0000, v132\n"
"v_lshlrev_b32 v116, 16, v133\n"
"v_and_b32 v117, 0xffff0000, v133\n"
"v_lshlrev_b32 v118, 16, v134\n"
"v_and_b32 v119, 0xffff0000, v134\n"
"v_lshlrev_b32 v120, 16, v135\n"
"v_and_b32 v121, 0xffff0000, v135\n"
"v_lshlrev_b32 v122, 16, v136\n"
"v_and_b32 v123, 0xffff0000, v136\n"
"v_lshlrev_b32 v124, 16, v137\n"
"v_and_b32 v125, 0xffff0000, v137\n"
"s_waitcnt lgkmcnt(0)\n"
"s_barrier\n"
"ds_read_b128 v[78:81], v37 offset:2048\n"
"ds_read_b128 v[82:85], v37 offset:3072\n"
"ds_read_b128 v[86:89], v37 offset:4096\n"
"ds_read_b128 v[90:93], v37 offset:5120\n"
"ds_read_b128 v[94:97], v38 offset:12288\n"
"ds_read_b128 v[98:101], v38 offset:13312\n"
"ds_read_b128 v[102:105], v38 offset:14336\n"
"ds_read_b128 v[106:109], v38 offset:15360\n"
"ds_read_b128 v[154:157], v41 offset:24576\n"
"ds_read_b128 v[158:161], v41 offset:25600\n"
"ds_read_b128 v[162:165], v41 offset:26624\n"
"ds_read_b128 v[166:169], v41 offset:27648\n"
"s_waitcnt lgkmcnt(11)\n"
"v_pk_add_f32 v[54:55], v[54:55], v[78:79]\n"
"v_pk_add_f32 v[56:57], v[56:57], v[80:81]\n"
"s_waitcnt lgkmcnt(10)\n"
"v_pk_add_f32 v[58:59], v[58:59], v[82:83]\n"
"v_pk_add_f32 v[60:61], v[60:61], v[84:85]\n"
"s_waitcnt lgkmcnt(9)\n"
"v_pk_add_f32 v[62:63], v[62:63], v[86:87]\n"
"v_pk_add_f32 v[64:65], v[64:65], v[88:89]\n"
"s_waitcnt lgkmcnt(8)\n"
"v_pk_add_f32 v[66:67], v[66:67], v[90:91]\n"
"v_pk_add_f32 v[68:69], v[68:69], v[92:93]\n"
"s_waitcnt lgkmcnt(7)\n"
"v_pk_add_f32 v[54:55], v[54:55], v[94:95]\n"
"v_pk_add_f32 v[56:57], v[56:57], v[96:97]\n"
"s_waitcnt lgkmcnt(6)\n"
"v_pk_add_f32 v[58:59], v[58:59], v[98:99]\n"
"v_pk_add_f32 v[60:61], v[60:61], v[100:101]\n"
"s_waitcnt lgkmcnt(5)\n"
"v_pk_add_f32 v[62:63], v[62:63], v[102:103]\n"
"v_pk_add_f32 v[64:65], v[64:65], v[104:105]\n"
"s_waitcnt lgkmcnt(4)\n"
"v_pk_add_f32 v[66:67], v[66:67], v[106:107]\n"
"v_pk_add_f32 v[68:69], v[68:69], v[108:109]\n"
"s_waitcnt lgkmcnt(3)\n"
"v_pk_add_f32 v[54:55], v[54:55], v[154:155]\n"
"v_pk_add_f32 v[56:57], v[56:57], v[156:157]\n"
"s_waitcnt lgkmcnt(2)\n"
"v_pk_add_f32 v[58:59], v[58:59], v[158:159]\n"
"v_pk_add_f32 v[60:61], v[60:61], v[160:161]\n"
"s_waitcnt lgkmcnt(1)\n"
"v_pk_add_f32 v[62:63], v[62:63], v[162:163]\n"
"v_pk_add_f32 v[64:65], v[64:65], v[164:165]\n"
"s_waitcnt lgkmcnt(0)\n"
"v_pk_add_f32 v[66:67], v[66:67], v[166:167]\n"
"v_pk_add_f32 v[68:69], v[68:69], v[168:169]\n"
"v_pk_fma_f32 v[54:55], v[54:55], v[138:139], v[110:111] op_sel_hi:[1,0,1]\n"
"v_pk_fma_f32 v[56:57], v[56:57], v[138:139], v[112:113] op_sel_hi:[1,0,1]\n"
"v_pk_fma_f32 v[58:59], v[58:59], v[138:139], v[114:115] op_sel_hi:[1,0,1]\n"
"v_pk_fma_f32 v[60:61], v[60:61], v[138:139], v[116:117] op_sel_hi:[1,0,1]\n"
"v_pk_fma_f32 v[62:63], v[62:63], v[138:139], v[118:119] op_sel_hi:[1,0,1]\n"
"v_pk_fma_f32 v[64:65], v[64:65], v[138:139], v[120:121] op_sel_hi:[1,0,1]\n"
"v_pk_fma_f32 v[66:67], v[66:67], v[138:139], v[122:123] op_sel_hi:[1,0,1]\n"
"v_pk_fma_f32 v[68:69], v[68:69], v[138:139], v[124:125] op_sel_hi:[1,0,1]\n"
"v_cvt_pk_bf16_f32 v130, v54, v55\n"
"v_cvt_pk_bf16_f32 v131, v56, v57\n"
"v_cvt_pk_bf16_f32 v132, v58, v59\n"
"v_cvt_pk_bf16_f32 v133, v60, v61\n"
"v_cvt_pk_bf16_f32 v134, v62, v63\n"
"v_cvt_pk_bf16_f32 v135, v64, v65\n"
"v_cvt_pk_bf16_f32 v136, v66, v67\n"
"v_cvt_pk_bf16_f32 v137, v68, v69\n"
"v_permlane32_swap_b32 v130, v132\n"
"v_permlane32_swap_b32 v131, v133\n"
"buffer_store_dwordx4 v[130:133], v25, s[16:19], s22 offen offset:0\n"
"v_permlane32_swap_b32 v134, v136\n"
"v_permlane32_swap_b32 v135, v137\n"
"buffer_store_dwordx4 v[134:137], v25, s[16:19], s22 offen offset:32\n"
"s_waitcnt vmcnt(0) lgkmcnt(0)\n"
"s_endpgm\n"
".Lretry1_%=:\n"
"s_mov_b32 s14, 0\n"
".Lloop1_%=:\n"
"s_nop 4\n"
"buffer_load_dwordx4 v[230:233], v4, s[8:11], s67 offen sc1\n"
"buffer_load_dwordx4 v[234:237], v4, s[8:11], s67 offen offset:1024 sc1\n"
"buffer_load_dwordx4 v[238:241], v52, s[8:11], s68 offen sc1\n"
"s_waitcnt vmcnt(0)\n"
"v_cmp_ne_u64 vcc, s[12:13], v[232:233]\n"
"v_cmp_ne_u64 s[30:31], s[12:13], v[236:237]\n"
"v_cmp_ne_u64 s[32:33], s[12:13], v[240:241]\n"
"s_nop 2\n"
"s_or_b64 vcc, vcc, s[30:31]\n"
"s_and_b64 s[32:33], s[32:33], s[74:75]\n"
"s_or_b64 vcc, vcc, s[32:33]\n"
"s_cbranch_vccz .Lback1_%=\n"
"s_add_u32 s14, s14, 1\n"
"s_cmp_lt_u32 s14, 40000\n"
"s_cbranch_scc1 .Lloop1_%=\n"
"s_branch .Lback1_%=\n"
".Lretry2_%=:\n"
"s_mov_b32 s14, 0\n"
".Lloop2_%=:\n"
"s_nop 4\n"
"buffer_load_dwordx2 v[50:51], v20, s[8:11], s7 offen sc1\n"
"s_waitcnt vmcnt(0)\n"
"v_cmp_ne_u64 vcc, s[12:13], v[50:51]\n"
"s_nop 4\n"
"s_cbranch_vccz .Lback2_%=\n"
"s_add_u32 s14, s14, 1\n"
"s_cmp_lt_u32 s14, 40000\n"
"s_cbranch_scc1 .Lloop2_%=\n"
"s_branch .Lback2_%=\n"
".Lretry3_%=:\n"
"s_mov_b32 s14, 0\n"
".Lloop3_%=:\n"
"s_nop 4\n"
"buffer_load_dwordx4 v[86:89], v16, s[8:11], s6 offen sc1\n"
"buffer_load_dwordx4 v[42:45], v4, s[8:11], s4 offen offset:0 sc1\n"
"buffer_load_dwordx4 v[46:49], v4, s[8:11], s4 offen offset:1024 sc1\n"
"buffer_load_dwordx4 v[54:57], v4, s[8:11], s4 offen offset:2048 sc1\n"
"buffer_load_dwordx4 v[62:65], v4, s[8:11], s4 offen offset:3072 sc1\n"
"buffer_load_dwordx4 v[66:69], v4, s[8:11], s5 offen offset:0 sc1\n"
"buffer_load_dwordx4 v[70:73], v4, s[8:11], s5 offen offset:1024 sc1\n"
"buffer_load_dwordx4 v[78:81], v4, s[8:11], s5 offen offset:2048 sc1\n"
"buffer_load_dwordx4 v[82:85], v4, s[8:11], s5 offen offset:3072 sc1\n"
"s_waitcnt vmcnt(0)\n"
"v_cmp_ne_u64 vcc, s[12:13], v[88:89]\n"
"v_cmp_ne_u64 s[30:31], s[12:13], v[44:45]\n"
"v_cmp_ne_u64 s[32:33], s[12:13], v[48:49]\n"
"v_cmp_ne_u64 s[34:35], s[12:13], v[56:57]\n"
"s_nop 1\n"
"s_or_b64 vcc, vcc, s[30:31]\n"
"s_or_b64 vcc, vcc, s[32:33]\n"
"s_or_b64 vcc, vcc, s[34:35]\n"
"v_cmp_ne_u64 s[28:29], s[12:13], v[64:65]\n"
"v_cmp_ne_u64 s[30:31], s[12:13], v[68:69]\n"
"v_cmp_ne_u64 s[32:33], s[12:13], v[72:73]\n"
"v_cmp_ne_u64 s[34:35], s[12:13], v[80:81]\n"
"s_nop 0\n"
"s_or_b64 vcc, vcc, s[28:29]\n"
"s_or_b64 vcc, vcc, s[30:31]\n"
"s_or_b64 vcc, vcc, s[32:33]\n"
"s_or_b64 vcc, vcc, s[34:35]\n"
"v_cmp_ne_u64 s[28:29], s[12:13], v[84:85]\n"
"s_nop 3\n"
"s_or_b64 vcc, vcc, s[28:29]\n"
"s_cbranch_vccz .Lback3_%=\n"
"s_add_u32 s14, s14, 1\n"
"s_cmp_lt_u32 s14, 40000\n"
"s_cbranch_scc1 .Lloop3_%=\n"
"s_branch .Lback3_%=\n"
".Lretry4_%=:\n"
"s_mov_b32 s14, 0\n"
".Lloop4_%=:\n"
"s_nop 4\n"
"buffer_load_dwordx2 v[34:35], v33, s[8:11], s17 offen sc1\n"
"s_waitcnt vmcnt(0)\n"
"v_cmp_ne_u64 vcc, s[12:13], v[34:35]\n"
"s_nop 4\n"
"s_cbranch_vccz .Lback4_%=\n"
"s_add_u32 s14, s14, 1\n"
"s_cmp_lt_u32 s14, 40000\n"
"s_cbranch_scc1 .Lloop4_%=\n"
"s_branch .Lback4_%=\n"
".Lretry5_%=:\n"
"s_mov_b32 s14, 0\n"
".Lloop5_%=:\n"
"s_nop 4\n"
"buffer_load_dwordx2 v[34:35], v18, s[8:11], s7 offen sc1\n"
"s_waitcnt vmcnt(0)\n"
"v_cmp_ne_u64 vcc, s[12:13], v[34:35]\n"
"s_nop 4\n"
"s_cbranch_vccz .Lback5_%=\n"
"s_add_u32 s14, s14, 1\n"
"s_cmp_lt_u32 s14, 40000\n"
"s_cbranch_scc1 .Lloop5_%=\n"
"s_branch .Lback5_%=\n"
".Lretry6_%=:\n"
"s_mov_b32 s14, 0\n"
".Lloop6_%=:\n"
"s_nop 4\n"
"buffer_load_dwordx2 v[34:35], v18, s[8:11], s26 offen sc1\n"
"s_waitcnt vmcnt(0)\n"
"v_cmp_ne_u64 vcc, s[12:13], v[34:35]\n"
"s_nop 4\n"
"s_cbranch_vccz .Lback6_%=\n"
"s_add_u32 s14, s14, 1\n"
"s_cmp_lt_u32 s14, 40000\n"
"s_cbranch_scc1 .Lloop6_%=\n"
"s_branch .Lback6_%=\n"
".Lend_%=:\n"
  :
  : "s"(__builtin_amdgcn_kernarg_segment_ptr()), "s"(blockIdx.x), "v"(threadIdx.x)
  : "v1","v2","v3","v4","v5","v6","v7","v8","v9","v10","v11","v12","v13","v14","v15","v16","v17","v18","v19","v20","v21","v22","v23","v24",
"v25","v26","v27","v28","v29","v30","v31","v32","v33","v34","v35","v36","v37","v38","v39","v40","v41","v42","v43","v44","v45","v46","v47","v48",
"v49","v50","v51","v52","v53","v54","v55","v56","v57","v58","v59","v60","v61","v62","v63","v64","v65","v66","v67","v68","v69","v70","v71","v72",
"v73","v74","v75","v76","v77","v78","v79","v80","v81","v82","v83","v84","v85","v86","v87","v88","v89","v90","v91","v92","v93","v94","v95","v96",
"v97","v98","v99","v100","v101","v102","v103","v104","v105","v106","v107","v108","v109","v110","v111","v112","v113","v114","v115","v116","v117","v118","v119","v120",
"v121","v122","v123","v124","v125","v126","v127","v128","v129","v130","v131","v132","v133","v134","v135","v136","v137","v138","v139","v140","v141","v142","v143","v144",
"v145","v146","v147","v148","v149","v150","v151","v152","v153","v154","v155","v156","v157","v158","v159","v160","v161","v162","v163","v164","v165","v166","v167","v168",
"v169","v170","v171","v172","v173","v174","v175","v176","v177","v178","v179","v180","v181","v182","v183","v184","v185","v186","v187","v188","v189","v190","v191","v192",
"v193","v194","v195","v196","v197","v198","v199","v200","v201","v202","v203","v204","v205","v206","v207","v208","v209","v210","v211","v212","v213","v214","v215","v216",
"v217","v218","v219","v220","v221","v222","v223","v224","v225","v226","v227","v228","v229","v230","v231","v232","v233","v234","v235","v236","v237","v238","v239","v240",
"v241","v242","v243","v244","v245","v246","v247","v248","v249","v250","v251","v252","v253","v254","v255","a0","a1","a2","a3","a4","a5","a6","a7","a8",
"a9","a10","a11","a12","a13","a14","a15","a16","a17","a18","a19","a20","a21","a22","a23","a24","a25","a26","a27","a28","a29","a30","a31","a32",
"a33","a34","a35","a36","a37","a38","a39","a40","a41","a42","a43","a44","a45","a46","a47","a48","a49","a50","a51","a52","a53","a54","a55","a56",
"a57","a58","a59","a60","a61","a62","a63","a64","a65","a66","a67","a68","a69","a70","a71","a72","a73","a74","a75","a76","a77","a78","a79","a80",
"a81","a82","a83","a84","a85","a86","a87","a88","a89","a90","a91","a92","a93","a94","a95","a96","a97","a98","a99","a100","a101","a102","a103","a104",
"a105","a106","a107","a108","a109","a110","a111","a112","a113","a114","a115","a116","a117","a118","a119","a120","a121","a122","a123","a124","a125","a126","a127","a128",
"a129","a130","a131","a132","a133","a134","a135","a136","a137","a138","a139","a140","a141","a142","a143","a144","a145","a146","a147","a148","a149","a150","a151","a152",
"a153","a154","a155","a156","a157","a158","a159","a160","a161","a162","a163","a164","a165","a166","a167","a168","a169","a170","a171","a172","a173","a174","a175","a176",
"a177","a178","a179","a180","a181","a182","a183","a184","a185","a186","a187","a188","a189","a190","a191","a192","a193","a194","a195","a196","a197","a198","a199","a200",
"a201","a202","a203","a204","a205","a206","a207","a208","a209","a210","a211","a212","a213","a214","a215","a216","a217","a218","a219","a220","a221","a222","a223","a224",
"a225","a226","a227","a228","a229","a230","a231","a232","a233","a234","a235","a236","a237","a238","a239","a240","a241","a242","a243","a244","a245","a246","a247","a248",
"a249","a250","a251","a252","a253","a254","a255","s3","s4","s5","s6","s7","s8","s9","s10","s11","s12","s13","s14","s15","s16","s17","s18","s19",
"s20","s21","s22","s23","s24","s25","s26","s27","s28","s29","s30","s31","s32","s33","s34","s35","s36","s37","s38","s39","s40","s41","s42","s43",
"s44","s45","s46","s47","s48","s49","s50","s51","s52","s53","s54","s55","s56","s57","s58","s59","s60","s61","s62","s63","s64","s65","s66","s67",
"s68","s69","s70","s71","s72","s73","s74","s75","s76","s77","s78","s79","s80","s81","s82","s83","s84","s85","s86","s87","s88","s89","s90","s91",
"s92","s93","s94","s95","s96","s97","s98","s99","s100","s101","vcc","scc","memory");
}

extern "C" void kernel_launch(void* const* d_in, const int* in_sizes, int n_in, void* d_out, int out_size, void* d_ws,
                              size_t ws_size, hipStream_t stream) {
  (void)in_sizes; (void)n_in; (void)out_size; (void)ws_size;
  (void)hipFuncSetAttribute((const void*)fused_block_kernel, hipFuncAttributeMaxDynamicSharedMemorySize, 163840);
  fused_block_kernel<<<256, 256, 163840, stream>>>(d_in[0], d_in[1], d_in[2], d_in[4], d_in[5], d_in[6], d_out, d_ws);
}
```
